```python
import torch

def _softmax(x, axis=-1):
    return torch.softmax(x, dim=axis)
def _gelu(x):
    return torch.nn.functional.gelu(x, approximate="tanh")
def _silu(x):
    return torch.nn.functional.silu(x)
def _sigmoid(x):
    return torch.sigmoid(x)
def _layernorm(x, axis=-1, eps=1e-5):
    m = x.mean(dim=axis, keepdim=True)
    v = x.var(dim=axis, keepdim=True, unbiased=False)
    return (x - m) / torch.sqrt(v + eps)
def _scatter_add(tgt, idx, src, axis=0):
    return tgt.clone().index_add_(axis, idx, src)
def _segment_sum(src, seg, n):
    out = torch.zeros((n,) + src.shape[1:], dtype=src.dtype)
    return out.index_add_(0, seg, src)
def _pad(x, pw):
    flat = tuple(v for pair in reversed(pw) for v in pair)
    return torch.nn.functional.pad(x, flat)

kernel_name = 'standalone__sequential_behavior_din__recsys'

def setup_inputs():

    _w = (lambda a: (lambda t: t.to(torch.bfloat16) if t.is_floating_point() else t)(torch.as_tensor(a)))
    B, L, D = 1024, 200, 128
    H = D
    return {
        "target": _w((lambda *s: torch.randn(*s, dtype=torch.bfloat16) * 1.6)(B, D)),
        "hist": _w((lambda *s: torch.randn(*s, dtype=torch.bfloat16) * 1.6)(B, L, D)),
        "mask": _w((lambda lo, hi, *s: torch.randint(lo, hi, s))(0, 2, B, L).to(float)),
        "w1": _w((lambda *s: torch.randn(*s, dtype=torch.bfloat16) * 1.6)(4 * D, H) * (4 * D) ** -0.5),
        "b1": _w((lambda *s: torch.randn(*s, dtype=torch.bfloat16) * 1.6)(H) * 0.1),
        "w2": _w((lambda *s: torch.randn(*s, dtype=torch.bfloat16) * 0.05)(H, 1) * H ** -0.5),
        "b2": _w((lambda *s: torch.randn(*s, dtype=torch.bfloat16) * 0.05)(1) * 0.1),
    }

def reference(target, hist, mask, w1, b1, w2, b2):
    L = hist.shape[1]
    tgt = (lambda x, n, axis: torch.repeat_interleave(x, n, dim=axis))(target[:, None, :], L, axis=1)
    feat = torch.concatenate([hist, tgt, hist - tgt, hist * tgt], axis=-1)
    h = _sigmoid(feat @ w1 + b1)
    scores = (h @ w2 + b2)[..., 0] * mask
    return torch.einsum('bl,bld->bd', scores, hist)
```

```cpp
#include <hip/hip_runtime.h>
#include <hip/hip_bf16.h>
#include <stdint.h>

static __device__ __forceinline__ float bf2f(uint16_t v) { return __uint_as_float(((uint32_t)v) << 16); }

constexpr int B = 1024, L = 200, D = 128, H = 128;

__global__ __launch_bounds__(256) void naive_kernel(const uint16_t* __restrict__ target, const uint16_t* __restrict__ hist,
    const uint16_t* __restrict__ mask, const uint16_t* __restrict__ w1, const uint16_t* __restrict__ b1,
    const uint16_t* __restrict__ w2, const uint16_t* __restrict__ b2, __hip_bfloat16* __restrict__ out) {
  __shared__ float weff[D][H];
  __shared__ float c[H];
  __shared__ float t[D];
  __shared__ float sc[L];
  __shared__ float red[256];
  int b = blockIdx.x, tid = threadIdx.x;
  if (tid < D) t[tid] = bf2f(target[b * D + tid]);
  __syncthreads();
  for (int i = tid; i < D * H; i += 256) {
    int k = i / H, h = i % H;
    weff[k][h] = bf2f(w1[k * H + h]) + bf2f(w1[(2 * D + k) * H + h]) + t[k] * bf2f(w1[(3 * D + k) * H + h]);
  }
  if (tid < H) {
    float s = bf2f(b1[tid]);
    for (int k = 0; k < D; ++k) s += t[k] * (bf2f(w1[(D + k) * H + tid]) - bf2f(w1[(2 * D + k) * H + tid]));
    c[tid] = s;
  }
  __syncthreads();
  int h = tid & 127, half = tid >> 7;
  float w2v = bf2f(w2[h]);
  for (int l0 = 0; l0 < L; l0 += 2) {
    int l = l0 + half;
    const uint16_t* row = hist + ((size_t)b * L + l) * D;
    float s = c[h];
    for (int k = 0; k < D; ++k) s += bf2f(row[k]) * weff[k][h];
    float sg = 1.f / (1.f + __expf(-s));
    red[tid] = sg * w2v;
    __syncthreads();
    if (h == 0) {
      float a = 0.f;
      for (int j = 0; j < 128; ++j) a += red[half * 128 + j];
      sc[l] = (a + bf2f(b2[0])) * bf2f(mask[b * L + l]);
    }
    __syncthreads();
  }
  if (tid < D) {
    float a = 0.f;
    for (int l = 0; l < L; ++l) a += sc[l] * bf2f(hist[((size_t)b * L + l) * D + tid]);
    out[b * D + tid] = __float2bfloat16(a);
  }
}

extern "C" void kernel_launch(void* const* d_in, const int* in_sizes, int n_in,
                              void* d_out, int out_size, void* d_ws, size_t ws_size,
                              hipStream_t stream) {
  naive_kernel<<<B, 256, 0, stream>>>((const uint16_t*)d_in[0], (const uint16_t*)d_in[1], (const uint16_t*)d_in[2],
      (const uint16_t*)d_in[3], (const uint16_t*)d_in[4], (const uint16_t*)d_in[5], (const uint16_t*)d_in[6],
      (__hip_bfloat16*)d_out);
}
```

```cpp
#include <hip/hip_runtime.h>
#include <hip/hip_bf16.h>
#include <stdint.h>

static __device__ __forceinline__ float bf2f(uint16_t v) { return __uint_as_float(((uint32_t)v) << 16); }

constexpr int B = 1024, L = 200, D = 128, H = 128;

__global__ __launch_bounds__(256) void naive_kernel(const uint16_t* __restrict__ target, const uint16_t* __restrict__ hist,
    const uint16_t* __restrict__ mask, const uint16_t* __restrict__ w1, const uint16_t* __restrict__ b1,
    const uint16_t* __restrict__ w2, const uint16_t* __restrict__ b2, __hip_bfloat16* __restrict__ out) {
  __shared__ float weff[D][H];
  __shared__ float c[H];
  __shared__ float t[D];
  __shared__ float sc[L];
  __shared__ float red[256];
  int b = blockIdx.x, tid = threadIdx.x;
  if (b < 256) { asm volatile("s_mov_b32 s40, 24\n"
"1:\n"
"v_fma_f32 v1, v2, v2, v3\n"
"v_fma_f32 v1, v2, v2, v3\n"
"v_fma_f32 v1, v2, v2, v3\n"
"v_fma_f32 v1, v2, v2, v3\n"
"v_fma_f32 v1, v2, v2, v3\n"
"v_fma_f32 v1, v2, v2, v3\n"
"v_fma_f32 v1, v2, v2, v3\n"
"v_fma_f32 v1, v2, v2, v3\n"
"v_fma_f32 v1, v2, v2, v3\n"
"v_fma_f32 v1, v2, v2, v3\n"
"v_fma_f32 v1, v2, v2, v3\n"
"v_fma_f32 v1, v2, v2, v3\n"
"v_fma_f32 v1, v2, v2, v3\n"
"v_fma_f32 v1, v2, v2, v3\n"
"v_fma_f32 v1, v2, v2, v3\n"
"v_fma_f32 v1, v2, v2, v3\n"
"v_fma_f32 v1, v2, v2, v3\n"
"v_fma_f32 v1, v2, v2, v3\n"
"v_fma_f32 v1, v2, v2, v3\n"
"v_fma_f32 v1, v2, v2, v3\n"
"v_fma_f32 v1, v2, v2, v3\n"
"v_fma_f32 v1, v2, v2, v3\n"
"v_fma_f32 v1, v2, v2, v3\n"
"v_fma_f32 v1, v2, v2, v3\n"
"v_fma_f32 v1, v2, v2, v3\n"
"v_fma_f32 v1, v2, v2, v3\n"
"v_fma_f32 v1, v2, v2, v3\n"
"v_fma_f32 v1, v2, v2, v3\n"
"v_fma_f32 v1, v2, v2, v3\n"
"v_fma_f32 v1, v2, v2, v3\n"
"v_fma_f32 v1, v2, v2, v3\n"
"v_fma_f32 v1, v2, v2, v3\n"
"v_fma_f32 v1, v2, v2, v3\n"
"v_fma_f32 v1, v2, v2, v3\n"
"v_fma_f32 v1, v2, v2, v3\n"
"v_fma_f32 v1, v2, v2, v3\n"
"v_fma_f32 v1, v2, v2, v3\n"
"v_fma_f32 v1, v2, v2, v3\n"
"v_fma_f32 v1, v2, v2, v3\n"
"v_fma_f32 v1, v2, v2, v3\n"
"v_fma_f32 v1, v2, v2, v3\n"
"v_fma_f32 v1, v2, v2, v3\n"
"v_fma_f32 v1, v2, v2, v3\n"
"v_fma_f32 v1, v2, v2, v3\n"
"v_fma_f32 v1, v2, v2, v3\n"
"v_fma_f32 v1, v2, v2, v3\n"
"v_fma_f32 v1, v2, v2, v3\n"
"v_fma_f32 v1, v2, v2, v3\n"
"v_fma_f32 v1, v2, v2, v3\n"
"v_fma_f32 v1, v2, v2, v3\n"
"v_fma_f32 v1, v2, v2, v3\n"
"v_fma_f32 v1, v2, v2, v3\n"
"v_fma_f32 v1, v2, v2, v3\n"
"v_fma_f32 v1, v2, v2, v3\n"
"v_fma_f32 v1, v2, v2, v3\n"
"v_fma_f32 v1, v2, v2, v3\n"
"v_fma_f32 v1, v2, v2, v3\n"
"v_fma_f32 v1, v2, v2, v3\n"
"v_fma_f32 v1, v2, v2, v3\n"
"v_fma_f32 v1, v2, v2, v3\n"
"v_fma_f32 v1, v2, v2, v3\n"
"v_fma_f32 v1, v2, v2, v3\n"
"v_fma_f32 v1, v2, v2, v3\n"
"v_fma_f32 v1, v2, v2, v3\n"
"v_fma_f32 v1, v2, v2, v3\n"
"v_fma_f32 v1, v2, v2, v3\n"
"v_fma_f32 v1, v2, v2, v3\n"
"v_fma_f32 v1, v2, v2, v3\n"
"v_fma_f32 v1, v2, v2, v3\n"
"v_fma_f32 v1, v2, v2, v3\n"
"v_fma_f32 v1, v2, v2, v3\n"
"v_fma_f32 v1, v2, v2, v3\n"
"v_fma_f32 v1, v2, v2, v3\n"
"v_fma_f32 v1, v2, v2, v3\n"
"v_fma_f32 v1, v2, v2, v3\n"
"v_fma_f32 v1, v2, v2, v3\n"
"v_fma_f32 v1, v2, v2, v3\n"
"v_fma_f32 v1, v2, v2, v3\n"
"v_fma_f32 v1, v2, v2, v3\n"
"v_fma_f32 v1, v2, v2, v3\n"
"v_fma_f32 v1, v2, v2, v3\n"
"v_fma_f32 v1, v2, v2, v3\n"
"v_fma_f32 v1, v2, v2, v3\n"
"v_fma_f32 v1, v2, v2, v3\n"
"v_fma_f32 v1, v2, v2, v3\n"
"v_fma_f32 v1, v2, v2, v3\n"
"v_fma_f32 v1, v2, v2, v3\n"
"v_fma_f32 v1, v2, v2, v3\n"
"v_fma_f32 v1, v2, v2, v3\n"
"v_fma_f32 v1, v2, v2, v3\n"
"v_fma_f32 v1, v2, v2, v3\n"
"v_fma_f32 v1, v2, v2, v3\n"
"v_fma_f32 v1, v2, v2, v3\n"
"v_fma_f32 v1, v2, v2, v3\n"
"v_fma_f32 v1, v2, v2, v3\n"
"v_fma_f32 v1, v2, v2, v3\n"
"v_fma_f32 v1, v2, v2, v3\n"
"v_fma_f32 v1, v2, v2, v3\n"
"v_fma_f32 v1, v2, v2, v3\n"
"v_fma_f32 v1, v2, v2, v3\n"
"v_fma_f32 v1, v2, v2, v3\n"
"v_fma_f32 v1, v2, v2, v3\n"
"v_fma_f32 v1, v2, v2, v3\n"
"v_fma_f32 v1, v2, v2, v3\n"
"v_fma_f32 v1, v2, v2, v3\n"
"v_fma_f32 v1, v2, v2, v3\n"
"v_fma_f32 v1, v2, v2, v3\n"
"v_fma_f32 v1, v2, v2, v3\n"
"v_fma_f32 v1, v2, v2, v3\n"
"v_fma_f32 v1, v2, v2, v3\n"
"v_fma_f32 v1, v2, v2, v3\n"
"v_fma_f32 v1, v2, v2, v3\n"
"v_fma_f32 v1, v2, v2, v3\n"
"v_fma_f32 v1, v2, v2, v3\n"
"v_fma_f32 v1, v2, v2, v3\n"
"v_fma_f32 v1, v2, v2, v3\n"
"v_fma_f32 v1, v2, v2, v3\n"
"v_fma_f32 v1, v2, v2, v3\n"
"v_fma_f32 v1, v2, v2, v3\n"
"v_fma_f32 v1, v2, v2, v3\n"
"v_fma_f32 v1, v2, v2, v3\n"
"v_fma_f32 v1, v2, v2, v3\n"
"v_fma_f32 v1, v2, v2, v3\n"
"v_fma_f32 v1, v2, v2, v3\n"
"v_fma_f32 v1, v2, v2, v3\n"
"v_fma_f32 v1, v2, v2, v3\n"
"v_fma_f32 v1, v2, v2, v3\n"
"v_fma_f32 v1, v2, v2, v3\n"
"s_sub_u32 s40, s40, 1\n"
"s_cmp_lg_u32 s40, 0\n"
"s_cbranch_scc1 1b\n" ::: "v1","v2","v3","s40","scc"); }
  if (tid < D) t[tid] = bf2f(target[b * D + tid]);
  __syncthreads();
  for (int i = tid; i < D * H; i += 256) {
    int k = i / H, h = i % H;
    weff[k][h] = bf2f(w1[k * H + h]) + bf2f(w1[(2 * D + k) * H + h]) + t[k] * bf2f(w1[(3 * D + k) * H + h]);
  }
  if (tid < H) {
    float s = bf2f(b1[tid]);
    for (int k = 0; k < D; ++k) s += t[k] * (bf2f(w1[(D + k) * H + tid]) - bf2f(w1[(2 * D + k) * H + tid]));
    c[tid] = s;
  }
  __syncthreads();
  int h = tid & 127, half = tid >> 7;
  float w2v = bf2f(w2[h]);
  for (int l0 = 0; l0 < L; l0 += 2) {
    int l = l0 + half;
    const uint16_t* row = hist + ((size_t)b * L + l) * D;
    float s = c[h];
    for (int k = 0; k < D; ++k) s += bf2f(row[k]) * weff[k][h];
    float sg = 1.f / (1.f + __expf(-s));
    red[tid] = sg * w2v;
    __syncthreads();
    if (h == 0) {
      float a = 0.f;
      for (int j = 0; j < 128; ++j) a += red[half * 128 + j];
      sc[l] = (a + bf2f(b2[0])) * bf2f(mask[b * L + l]);
    }
    __syncthreads();
  }
  if (tid < D) {
    float a = 0.f;
    for (int l = 0; l < L; ++l) a += sc[l] * bf2f(hist[((size_t)b * L + l) * D + tid]);
    out[b * D + tid] = __float2bfloat16(a);
  }
}

extern "C" void kernel_launch(void* const* d_in, const int* in_sizes, int n_in,
                              void* d_out, int out_size, void* d_ws, size_t ws_size,
                              hipStream_t stream) {
  naive_kernel<<<B, 256, 0, stream>>>((const uint16_t*)d_in[0], (const uint16_t*)d_in[1], (const uint16_t*)d_in[2],
      (const uint16_t*)d_in[3], (const uint16_t*)d_in[4], (const uint16_t*)d_in[5], (const uint16_t*)d_in[6],
      (__hip_bfloat16*)d_out);
}
```

```cpp
#include <hip/hip_runtime.h>
#include <hip/hip_bf16.h>

__global__ __launch_bounds__(256, 1) void din_kernel(const void* target, const void* hist, const void* mask, const void* w1,
                                                     const void* b1, const void* w2, const void* b2, void* out) {
  __shared__ __attribute__((aligned(16))) char smem[163840];
  const unsigned wave = __builtin_amdgcn_readfirstlane(threadIdx.x >> 6);
  const unsigned bid = blockIdx.x;
  asm volatile(
    "s_load_dwordx16 s[8:23], %0, 0x0\n"
    "s_getpc_b64 s[96:97]\n"
    "L_pc_%=:\n"
    "v_mbcnt_lo_u32_b32 v254, -1, 0\n"
    "v_mbcnt_hi_u32_b32 v254, -1, v254\n"
    "s_mov_b32 s44, %2\n"
    "s_lshr_b32 s46, %1, 1\n"
    "s_lshl_b32 s45, %1, 2\n"
    "s_and_b32 s46, s46, 0x7c\n"
    "s_add_u32 s46, s46, s44\n"
    "s_and_b32 s47, s46, 15\n"
    "s_mul_i32 s47, s47, 2048\n"
    "v_lshl_add_u32 v96, v254, 7, s47\n"
    "v_min_u32 v96, L_cpfend_%=-L_pc_%=, v96\n"
    "s_mov_b64 exec, 0xffff\n"
    "global_load_dword a160, v96, s[96:97]\n"
    "s_mov_b64 exec, -1\n"
    "s_mul_i32 s80, s44, 40960\n"
    "s_lshl_b32 s81, s44, 6\n"
    "s_lshl_b32 s82, s45, 8\n"
    "s_mul_i32 s83, s45, 51200\n"
    "s_add_u32 s45, s45, s44\n"
    "s_mul_i32 s79, s45, 400\n"
    "v_lshlrev_b32 v96, 3, v254\n"
    "v_lshlrev_b32 v97, 4, v254\n"
    "v_lshrrev_b32 v98, 2, v254\n"
    "v_lshlrev_b32 v98, 8, v98\n"
    "v_and_b32 v99, 3, v254\n"
    "v_lshl_add_u32 v98, v99, 4, v98\n"
    "v_add_u32 v251, s81, v98\n"
    "s_and_b32 s46, s46, 127\n"
    "s_mul_i32 s46, s46, 1024\n"
    "v_lshl_add_u32 v101, v254, 7, s46\n"
    "s_mov_b32 s26, 0x20000\n"
    "s_mov_b32 s27, 0x20000\n"
    "s_mov_b32 s30, 0x190\n"
    "s_mov_b32 s31, 0x20000\n"
    "s_mov_b32 s38, 0x400\n"
    "s_mov_b32 s39, 0x20000\n"
    "s_mov_b32 s53, 0x17c00\n"
    "s_mov_b32 s54, 0xf800\n"
    "s_mov_b32 s55, 0x7400\n"
    "v_mov_b32 v100, 0\n"
    "v_mov_b32 v202, 0\n"
    "v_mov_b32 v203, 0\n"
    "v_mov_b32 v200, 0\n"
    "v_and_b32 v103, 31, v254\n"
    "v_lshl_add_u32 v103, v103, 1, s81\n"
    "s_mul_i32 s46, s44, 51200\n"
    "s_add_u32 s46, s46, 3072\n"
    "v_lshl_add_u32 v104, v254, 14, s46\n"
    "s_add_u32 s46, s80, 0x1e00\n"
    "v_lshlrev_b32 v145, 2, v254\n"
    "v_add_u32 v144, s46, v145\n"
    "v_mov_b32 v146, -1\n"
    "ds_write_b32 v144, v146\n"
    "v_subrev_u32 v147, 0x1e0, v144\n"
    "ds_write_b32 v147, v100\n"
    "v_mov_b32 v144, s46\n"
    "v_add_u32 v146, 1, v145\n"
    "v_add_u32 v147, 2, v145\n"
    "v_add_u32 v148, 3, v145\n"
    "s_waitcnt lgkmcnt(0)\n"
    "s_mov_b32 s24, s14\n"
    "s_mov_b32 s25, s15\n"
    "s_add_u32 s28, s12, s79\n"
    "s_addc_u32 s29, s13, 0\n"
    "buffer_load_dwordx2 v[224:225], v96, s[28:31], 0 offen nt\n"
    "s_add_u32 s36, s8, s82\n"
    "s_addc_u32 s37, s9, 0\n"
    "s_add_u32 m0, s80, 0x1800\n"
    "s_nop 0\n"
    "buffer_load_dwordx4 v97, s[36:39], 0 offen nt lds\n"
    "s_sub_u32 s83, s83, 3072\n"
    "s_ashr_i32 s78, s83, 31\n"
    "s_add_u32 s84, s10, s83\n"
    "s_addc_u32 s85, s11, s78\n"
    "s_add_u32 s40, s22, s82\n"
    "s_addc_u32 s41, s23, 0\n"
    "s_mov_b64 exec, 0xff\n"
    "buffer_load_dword a161, v101, s[24:27], 0 offen\n"
    "s_mov_b64 exec, -1\n"
    "global_load_dword v199, v145, s[18:19]\n"
    "global_load_short_d16_hi v200, v103, s[18:19]\n"
    "s_mov_b32 exec_hi, 0\n"
    "global_load_short_d16_hi v202, v103, s[16:17]\n"
    "global_load_short_d16_hi v203, v100, s[20:21]\n"
    "s_mov_b32 exec_hi, -1\n"
    "s_add_u32 m0, s80, 0x2000\n"
    "s_nop 0\n"
    "buffer_load_dwordx4 v251, s[24:27], 0 offen offset:0 lds\n"
    "s_mov_b32 s69, 0x0c020105\n"
    "s_mov_b32 s68, 0x3f803f80\n"
    "s_mov_b32 s56, 0xffff\n"
    "v_bfe_u32 v246, v254, 2, 3\n"
    "s_add_u32 s47, s81, 0x1e00\n"
    "v_lshl_add_u32 v246, v246, 3, s47\n"
    "v_add_u32 v247, 0x14000, v246\n"
    "v_lshrrev_b32 v102, 5, v254\n"
    "v_bfe_u32 v111, v254, 4, 1\n"
    "v_bfe_u32 v112, v254, 2, 2\n"
    "s_and_b32 s47, s44, 1\n"
    "s_lshl_b32 s47, s47, 5\n"
    "v_xor_b32 v119, v99, v111\n"
    "v_lshlrev_b32 v119, 4, v119\n"
    "v_lshl_add_u32 v241, v102, 6, v119\n"
    "v_xor_b32 v241, s47, v241\n"
    "s_add_u32 s88, s84, 51200\n"
    "s_addc_u32 s89, s85, 0\n"
    "s_add_u32 s92, s88, 51200\n"
    "s_addc_u32 s93, s89, 0\n"
    "buffer_load_dwordx4 v251, s[24:27], s53 offen offset:1024 lds\n"
    "s_add_u32 s96, s92, 51200\n"
    "s_addc_u32 s97, s93, 0\n"
    "s_mov_b32 s86, 0xd400\n"
    "s_mov_b32 s87, 0x20000\n"
    "s_mov_b64 s[90:91], s[86:87]\n"
    "s_mov_b64 s[94:95], s[86:87]\n"
    "s_mov_b64 s[98:99], s[86:87]\n"
    "s_movk_i32 s48, 0xc00\n"
    "s_movk_i32 s49, 0x880\n"
    "s_movk_i32 s50, 0x400\n"
    "s_movk_i32 s51, 0x80\n"
    "s_mov_b32 s60, 0xc0c0400\n"
    "s_mov_b32 s61, 0xc0c0500\n"
    "s_mov_b32 s62, 0xc0c0600\n"
    "s_mov_b32 s63, 0xc0c0700\n"
    "v_and_b32 v153, 0x7c, v145\n"
    "v_add_u32 v153, v153, v144\n"
    "v_subrev_u32 v153, 0xc0, v153\n"
    "v_lshl_add_u32 v255, v102, 4, s80\n"
    "v_mov_b32 v201, s44\n"
    "buffer_load_dwordx4 v251, s[24:27], s54 offen offset:2048 lds\n"
    "v_add_u32 v149, 0x100, v144\n"
    "v_add_u32 v149, v149, v145\n"
    "v_bfe_u32 v114, v254, 1, 1\n"
    "v_bfe_u32 v115, v254, 3, 1\n"
    "v_and_b32 v116, 1, v254\n"
    "v_bfe_u32 v117, v254, 2, 1\n"
    "v_lshl_add_u32 v120, v111, 1, v114\n"
    "v_lshl_add_u32 v113, v120, 8, s80\n"
    "v_lshl_add_u32 v113, v115, 5, v113\n"
    "v_lshl_add_u32 v113, v116, 4, v113\n"
    "v_lshl_add_u32 v113, v117, 3, v113\n"
    "v_bfe_u32 v121, v254, 1, 2\n"
    "v_lshl_add_u32 v118, v121, 8, s80\n"
    "v_lshl_add_u32 v118, v102, 3, v118\n"
    "v_xor_b32 v119, v116, v111\n"
    "v_or_b32 v119, v119, v115\n"
    "v_cmp_eq_u32 vcc, 0, v119\n"
    "v_subrev_u32 v119, 0x19e0, v144\n"
    "s_nop 0\n"
    "buffer_load_dwordx4 v251, s[24:27], s55 offen offset:3072 lds\n"
    "s_add_u32 m0, s80, 0x3000\n"
    "v_add_u32 v251, 0x1000, v251\n"
    "v_cndmask_b32 v252, v119, v118, vcc\n"
    "v_lshlrev_b32 v119, 3, v99\n"
    "v_lshl_add_u32 v119, v111, 5, v119\n"
    "v_lshl_add_u32 v119, v112, 6, v119\n"
    "s_add_u32 s47, s80, 8192\n"
    "v_add_u32 v119, s47, v119\n"
    "v_lshl_add_u32 v249, v102, 10, v119\n"
    "v_lshl_add_u32 v250, v102, 8, v119\n"
    "v_add_u32 v250, 0x800, v250\n"
    "s_mov_b32 s32, -1\n"
    "s_mov_b32 s33, 0\n"
    "v_mov_b32 v124, 0x0c0c0100\n"
    "v_mov_b32 v119, 0x0c0c0504\n"
    "v_mov_b32 v125, 0x0c0c0706\n"
    "v_cndmask_b32 v119, v119, v124, s[32:33]\n"
    "v_cndmask_b32 v125, v125, v124, s[32:33]\n"
    "v_lshlrev_b32 v124, 4, v116\n"
    "v_alignbit_b32 v119, v119, v119, v124\n"
    "v_alignbit_b32 v125, v125, v125, v124\n"
    "buffer_load_dwordx4 v251, s[24:27], 0 offen offset:0 lds\n"
    "v_mov_b32 v124, 0x0c0c0c0c\n"
    "v_mov_b32 v126, s68\n"
    "v_cmp_eq_u32 s[70:71], 0, v121\n"
    "v_cmp_eq_u32 s[72:73], 1, v121\n"
    "v_cmp_eq_u32 s[74:75], 2, v121\n"
    "v_cmp_eq_u32 s[76:77], 3, v121\n"
    "v_cndmask_b32 v184, v124, v119, s[70:71]\n"
    "v_cndmask_b32 v188, v124, v125, s[70:71]\n"
    "v_cndmask_b32 v185, v124, v119, s[72:73]\n"
    "v_cndmask_b32 v189, v124, v125, s[72:73]\n"
    "v_cndmask_b32 v186, v124, v119, s[74:75]\n"
    "v_cndmask_b32 v190, v124, v125, s[74:75]\n"
    "v_cndmask_b32 v187, v124, v119, s[76:77]\n"
    "v_cndmask_b32 v191, v124, v125, s[76:77]\n"
    "v_perm_b32 v124, v126, v126, v184\n"
    "v_cndmask_b32 v176, 0, v124, s[32:33]\n"
    "v_cndmask_b32 v180, v124, 0, s[32:33]\n"
    "v_perm_b32 v124, v126, v126, v185\n"
    "buffer_load_dwordx4 v251, s[24:27], s53 offen offset:1024 lds\n"
    "v_cndmask_b32 v177, 0, v124, s[32:33]\n"
    "v_cndmask_b32 v181, v124, 0, s[32:33]\n"
    "v_perm_b32 v124, v126, v126, v186\n"
    "v_cndmask_b32 v178, 0, v124, s[32:33]\n"
    "v_cndmask_b32 v182, v124, 0, s[32:33]\n"
    "v_perm_b32 v124, v126, v126, v187\n"
    "v_cndmask_b32 v179, 0, v124, s[32:33]\n"
    "v_cndmask_b32 v183, v124, 0, s[32:33]\n"
    "s_mov_b32 s66, 0xbfb8aa3b\n"
    "v_bfe_u32 v119, v254, 3, 2\n"
    "v_mul_u32_u24 v119, 0xa000, v119\n"
    "v_and_b32 v124, 7, v254\n"
    "v_lshl_add_u32 v119, v124, 6, v119\n"
    "v_xor_b32 v125, v102, v112\n"
    "v_lshl_add_u32 v226, v125, 4, v119\n"
    "v_xor_b32 v227, 32, v226\n"
    "v_lshrrev_b32 v125, 4, v254\n"
    "v_mul_u32_u24 v119, 0xa000, v125\n"
    "v_lshl_add_u32 v119, v112, 6, v119\n"
    "buffer_load_dwordx4 v251, s[24:27], s54 offen offset:2048 lds\n"
    "v_lshl_add_u32 v119, v116, 3, v119\n"
    "s_lshl_b32 s47, s44, 9\n"
    "v_add_u32 v119, s47, v119\n"
    "v_lshl_add_u32 v228, v120, 4, v119\n"
    "v_xor_b32 v229, 16, v228\n"
    "v_add_u32 v229, 0x100, v229\n"
    "v_xor_b32 v230, 32, v228\n"
    "v_xor_b32 v231, 32, v229\n"
    "v_and_b32 v119, 31, v254\n"
    "v_lshlrev_b32 v119, 1, v119\n"
    "s_mul_i32 s47, s44, 544\n"
    "s_add_u32 s47, s47, 0x8000\n"
    "v_mul_u32_u24 v126, 272, v102\n"
    "v_add3_u32 v232, v119, v126, s47\n"
    "v_add_u32 v233, 0x14000, v232\n"
    "v_lshl_add_u32 v240, v102, 8, v119\n"
    "v_add_u32 v240, s81, v240\n"
    "v_lshlrev_b32 v125, 4, v125\n"
    "v_add_u32 v125, 0x8000, v125\n"
    "buffer_load_dwordx4 v251, s[24:27], s55 offen offset:3072 lds\n"
    "s_add_u32 m0, s80, 0x4000\n"
    "v_add_u32 v251, 0x1000, v251\n"
    "v_mul_u32_u24 v124, 272, v124\n"
    "v_cmp_eq_u32 vcc, 0, v115\n"
    "v_mov_b32 v126, 2176\n"
    "s_nop 0\n"
    "v_cndmask_b32 v119, v126, v124, vcc\n"
    "v_cndmask_b32 v124, v124, v126, vcc\n"
    "v_add_u32 v234, v125, v119\n"
    "v_add_u32 v125, 0xa000, v125\n"
    "v_add_u32 v235, v125, v124\n"
    "v_add_u32 v236, 0x14000, v234\n"
    "v_add_u32 v237, 0x14000, v235\n"
    "s_movk_i32 s42, 1024\n"
    "s_mov_b32 s43, 0x20000\n"
    "s_add_u32 s47, s80, 0x8000\n"
    "v_lshrrev_b32 v124, 4, v254\n"
    "v_and_b32 v125, 15, v254\n"
    "v_lshl_add_u32 v238, v124, 8, s47\n"
    "v_lshl_add_u32 v238, v125, 2, v238\n"
    "v_lshl_add_u32 v239, v102, 4, s47\n"
    "v_lshl_add_u32 v253, v254, 2, s47\n"
    "s_add_u32 s81, s80, 0x8000\n"
    "s_waitcnt vmcnt(14)\n"
    "v_and_b32 v136, 0x7fff7fff, v224\n"
    "v_and_b32 v137, 0x7fff7fff, v225\n"
    "v_cmp_ne_u16 s[70:71], 0, v136\n"
    "v_cmp_lt_u32 s[72:73], s56, v136\n"
    "v_cmp_ne_u16 s[74:75], 0, v137\n"
    "v_cmp_lt_u32 s[76:77], s56, v137\n"
    "v_mbcnt_lo_u32_b32 v128, s70, 0\n"
    "v_mbcnt_hi_u32_b32 v128, s71, v128\n"
    "v_mbcnt_lo_u32_b32 v128, s72, v128\n"
    "v_mbcnt_hi_u32_b32 v128, s73, v128\n"
    "v_mbcnt_lo_u32_b32 v128, s74, v128\n"
    "v_mbcnt_hi_u32_b32 v128, s75, v128\n"
    "v_mbcnt_lo_u32_b32 v128, s76, v128\n"
    "v_mbcnt_hi_u32_b32 v128, s77, v128\n"
    "v_mul_u32_u24 v138, 0x808, v128\n"
    "v_addc_co_u32 v129, vcc, 0, v128, s[70:71]\n"
    "v_perm_b32 v138, v138, v144, s69\n"
    "v_mul_u32_u24 v139, 0x808, v129\n"
    "v_addc_co_u32 v130, vcc, 0, v129, s[72:73]\n"
    "v_perm_b32 v139, v139, v144, s69\n"
    "v_mul_u32_u24 v140, 0x808, v130\n"
    "v_addc_co_u32 v131, vcc, 0, v130, s[74:75]\n"
    "v_perm_b32 v140, v140, v144, s69\n"
    "v_mul_u32_u24 v141, 0x808, v131\n"
    "v_perm_b32 v141, v141, v144, s69\n"
    "v_cndmask_b32 v138, v149, v138, s[70:71]\n"
    "v_cndmask_b32 v139, v149, v139, s[72:73]\n"
    "v_cndmask_b32 v140, v149, v140, s[74:75]\n"
    "v_cndmask_b32 v141, v149, v141, s[76:77]\n"
    "ds_write_b8 v138, v145\n"
    "ds_write_b8 v139, v146\n"
    "ds_write_b8 v140, v147\n"
    "ds_write_b8 v141, v148\n"
    "v_xor_b32 v142, s68, v224\n"
    "v_xor_b32 v143, s68, v225\n"
    "v_readlane_b32 s78, v128, 63\n"
    "v_pk_min_u16 v142, v142, v136\n"
    "v_pk_min_u16 v143, v143, v137\n"
    "v_or_b32 v142, v142, v143\n"
    "v_cmp_ne_u32 vcc, 0, v142\n"
    "s_cmp_gt_u32 s78, 160\n"
    "s_cselect_b32 s46, 2, 0\n"
    "s_cmp_gt_u32 s78, 128\n"
    "s_addc_u32 s46, s46, 0\n"
    "s_lshl_b32 s46, s46, 1\n"
    "s_cmp_lg_u64 vcc, 0\n"
    "s_addc_u32 s46, s46, 0\n"
    "v_mov_b32 v142, s46\n"
    "s_mov_b64 exec, 1\n"
    "ds_write_b8 v201, v142 offset:7616\n"
    "s_mov_b64 exec, -1\n"
    "s_waitcnt vmcnt(13)\n"
    "ds_read_b64_tr_b16 v[208:209], v113 offset:6144\n"
    "ds_read_b64_tr_b16 v[210:211], v113 offset:6208\n"
    "ds_read_b64_tr_b16 v[212:213], v113 offset:6272\n"
    "ds_read_b64_tr_b16 v[214:215], v113 offset:6336\n"
    "ds_read_b64 a[92:93], v252 offset:6144\n"
    "ds_read_b64 a[94:95], v252 offset:6160\n"
    "s_waitcnt vmcnt(11)\n"
    "v_and_b32 v150, 0x7fff7fff, v199\n"
    "v_lshlrev_b32 v151, 16, v150\n"
    "v_max_u32 v150, v150, v151\n"
    "s_waitcnt vmcnt(10)\n"
    "v_and_b32 v192, 0x80000000, v200\n"
    "v_min_f32 v151, 0, v200\n"
    "v_max_u32_dpp v150, v150, v150 row_shr:1 row_mask:0xf bank_mask:0xf\n"
    "v_xor_b32 v192, s66, v192\n"
    "buffer_load_dwordx4 v251, s[24:27], 0 offen offset:0 lds\n"
    "v_add_f32_dpp v151, v151, v151 quad_perm:[1,0,3,2] row_mask:0xf bank_mask:0xf\n"
    "v_max_u32_dpp v150, v150, v150 row_shr:2 row_mask:0xf bank_mask:0xf\n"
    "v_mov_b32 v194, v192\n"
    "v_add_f32_dpp v151, v151, v151 quad_perm:[2,3,0,1] row_mask:0xf bank_mask:0xf\n"
    "v_max_u32_dpp v150, v150, v150 row_shr:4 row_mask:0xf bank_mask:0xf\n"
    "v_mov_b32 v195, v192\n"
    "v_add_f32_dpp v151, v151, v151 row_ror:8 row_mask:0xf bank_mask:0xf\n"
    "v_max_u32_dpp v150, v150, v150 row_shr:8 row_mask:0xf bank_mask:0xf\n"
    "buffer_load_dwordx4 v251, s[24:27], s53 offen offset:1024 lds\n"
    "v_permlane16_swap_b32 v194, v195\n"
    "v_mov_b32 v152, v151\n"
    "v_max_u32_dpp v150, v150, v150 row_bcast:15 row_mask:0xa bank_mask:0xf\n"
    "s_nop 0\n"
    "v_permlane16_swap_b32 v151, v152\n"
    "s_waitcnt vmcnt(6)\n"
    "ds_read_b64_tr_b16 a[124:125], v249 offset:0\n"
    "ds_read_b64_tr_b16 a[126:127], v249 offset:512\n"
    "buffer_load_dwordx4 v251, s[24:27], s54 offen offset:2048 lds\n"
    "ds_read_b64_tr_b16 a[132:133], v250 offset:0\n"
    "ds_read_b64_tr_b16 a[134:135], v250 offset:512\n"
    "ds_read_b64_tr_b16 a[128:129], v249 offset:256\n"
    "ds_read_b64_tr_b16 a[130:131], v249 offset:768\n"
    "ds_read_b64_tr_b16 a[136:137], v250 offset:1024\n"
    "ds_read_b64_tr_b16 a[138:139], v250 offset:1536\n"
    "v_max_u32_dpp v150, v150, v150 row_bcast:31 row_mask:0xc bank_mask:0xf\n"
    "v_add_f32 v151, v151, v152\n"
    "buffer_load_dwordx4 v251, s[24:27], s55 offen offset:3072 lds\n"
    "s_add_u32 m0, s80, 0x5000\n"
    "v_add_u32 v251, 0x1000, v251\n"
    "s_cmp_eq_u32 s44, 0\n"
    "s_cselect_b32 s57, 1.0, 0\n"
    "v_readlane_b32 s46, v150, 63\n"
    "v_readlane_b32 s47, v151, 0\n"
    "v_readlane_b32 s82, v151, 4\n"
    "s_lshr_b32 s46, s46, 23\n"
    "s_max_u32 s46, s46, 64\n"
    "v_mov_b32 v198, s47\n"
    "buffer_load_dwordx4 v251, s[24:27], 0 offen offset:0 lds\n"
    "s_sub_u32 s83, s46, 39\n"
    "s_lshl_b32 s83, s83, 23\n"
    "v_mov_b32 v152, s82\n"
    "v_max_f32_e64 v150, |v200|, s83\n"
    "s_sub_u32 s47, s46, 156\n"
    "v_log_f32 v150, v150\n"
    "v_cvt_f32_i32 v151, s47\n"
    "s_sub_u32 s64, s46, 29\n"
    "buffer_load_dwordx4 v251, s[24:27], s53 offen offset:1024 lds\n"
    "s_lshl_b32 s64, s64, 23\n"
    "v_cndmask_b32 v198, v152, v198, s[32:33]\n"
    "v_sub_f32 v150, v151, v150\n"
    "s_sub_u32 s65, 283, s46\n"
    "s_lshl_b32 s65, s65, 23\n"
    "v_exp_f32 v151, v150\n"
    "v_mov_b32 v196, v150\n"
    "ds_write_b32 v153, v151\n"
    "buffer_load_dwordx4 v251, s[24:27], s54 offen offset:2048 lds\n"
    "s_waitcnt lgkmcnt(14)\n"
    "v_perm_b32 v160, v208, s68, v184\n"
    "v_perm_b32 v161, v208, s68, v185\n"
    "v_perm_b32 v162, v208, s68, v186\n"
    "v_perm_b32 v163, v208, s68, v187\n"
    "v_perm_b32 v164, v208, s68, v188\n"
    "v_perm_b32 v165, v208, s68, v189\n"
    "v_perm_b32 v166, v208, s68, v190\n"
    "v_perm_b32 v167, v208, s68, v191\n"
    "buffer_load_dwordx4 v251, s[24:27], s55 offen offset:3072 lds\n"
    "s_add_u32 m0, s80, 0x6000\n"
    "v_add_u32 v251, 0x1000, v251\n"
    "s_waitcnt lgkmcnt(1)\n"
    "v_mfma_f32_32x32x16_bf16 v[96:111], v[160:163], a[124:127], 0\n"
    "ds_read_b64 a[96:97], v252 offset:6176\n"
    "ds_read_b64 a[98:99], v252 offset:6192\n"
    "buffer_load_dwordx4 v251, s[24:27], 0 offen offset:0 lds\n"
    "v_perm_b32 v168, v209, s68, v184\n"
    "v_mfma_f32_32x32x16_bf16 v[96:111], v[176:179], a[132:135], v[96:111]\n"
    "buffer_load_dwordx4 v251, s[24:27], s53 offen offset:1024 lds\n"
    "v_perm_b32 v169, v209, s68, v185\n"
    "v_mfma_f32_32x32x16_bf16 v[112:127], v[164:167], a[128:131], 0\n"
    "s_waitcnt vmcnt(10)\n"
    "ds_read_b64_tr_b16 a[140:141], v249 offset:4096\n"
    "ds_read_b64_tr_b16 a[142:143], v249 offset:4608\n"
    "buffer_load_dwordx4 v251, s[24:27], s54 offen offset:2048 lds\n"
    "v_perm_b32 v170, v209, s68, v186\n"
    "v_perm_b32 v171, v209, s68, v187\n"
    "v_mfma_f32_32x32x16_bf16 v[112:127], v[180:183], a[132:135], v[112:127]\n"
    "ds_read_b64_tr_b16 a[148:149], v250 offset:4096\n"
    "ds_read_b64_tr_b16 a[150:151], v250 offset:4608\n"
    "buffer_load_dwordx4 v251, s[24:27], s55 offen offset:3072 lds\n"
    "s_add_u32 m0, s80, 0x7000\n"
    "v_add_u32 v251, 0x1000, v251\n"
    "v_perm_b32 v172, v209, s68, v188\n"
    "v_mfma_f32_16x16x32_bf16 v[220:223], a[92:95], a[132:135], 0\n"
    "ds_read_b64_tr_b16 a[144:145], v249 offset:4352\n"
    "ds_read_b64_tr_b16 a[146:147], v249 offset:4864\n"
    "buffer_load_dwordx4 v251, s[24:27], 0 offen offset:0 lds\n"
    "v_perm_b32 v173, v209, s68, v189\n"
    "v_mfma_f32_16x16x32_bf16 v[216:219], a[92:95], a[136:139], 0\n"
    "ds_read_b64_tr_b16 a[152:153], v250 offset:5120\n"
    "ds_read_b64_tr_b16 a[154:155], v250 offset:5632\n"
    "buffer_load_dwordx4 v251, s[24:27], s53 offen offset:1024 lds\n"
    "v_perm_b32 v174, v209, s68, v190\n"
    "v_perm_b32 v175, v209, s68, v191\n"
    "v_pk_mul_f32 v[96:97], v[96:97], v[192:193] op_sel_hi:[1,0]\n"
    "v_pk_mul_f32 v[98:99], v[98:99], v[192:193] op_sel_hi:[1,0]\n"
    "v_pk_mul_f32 v[100:101], v[100:101], v[192:193] op_sel_hi:[1,0]\n"
    "v_pk_mul_f32 v[102:103], v[102:103], v[192:193] op_sel_hi:[1,0]\n"
    "v_pk_mul_f32 v[104:105], v[104:105], v[192:193] op_sel_hi:[1,0]\n"
    "v_pk_mul_f32 v[106:107], v[106:107], v[192:193] op_sel_hi:[1,0]\n"
    "v_pk_mul_f32 v[108:109], v[108:109], v[192:193] op_sel_hi:[1,0]\n"
    "v_pk_mul_f32 v[110:111], v[110:111], v[192:193] op_sel_hi:[1,0]\n"
    "v_pk_mul_f32 v[112:113], v[112:113], v[192:193] op_sel_hi:[1,0]\n"
    "v_pk_mul_f32 v[114:115], v[114:115], v[192:193] op_sel_hi:[1,0]\n"
    "v_pk_mul_f32 v[116:117], v[116:117], v[192:193] op_sel_hi:[1,0]\n"
    "v_pk_mul_f32 v[118:119], v[118:119], v[192:193] op_sel_hi:[1,0]\n"
    "v_pk_mul_f32 v[120:121], v[120:121], v[192:193] op_sel_hi:[1,0]\n"
    "v_pk_mul_f32 v[122:123], v[122:123], v[192:193] op_sel_hi:[1,0]\n"
    "v_pk_mul_f32 v[124:125], v[124:125], v[192:193] op_sel_hi:[1,0]\n"
    "v_pk_mul_f32 v[126:127], v[126:127], v[192:193] op_sel_hi:[1,0]\n"
    "s_waitcnt lgkmcnt(0)\n"
    "v_mfma_f32_32x32x16_bf16 v[128:143], v[168:171], a[140:143], 0\n"
    "ds_read_b64 a[92:93], v252 offset:6208\n"
    "ds_read_b64 a[94:95], v252 offset:6224\n"
    "buffer_load_dwordx4 v251, s[24:27], s54 offen offset:2048 lds\n"
    "v_perm_b32 v160, v210, s68, v184\n"
    "v_perm_b32 v161, v210, s68, v185\n"
    "v_perm_b32 v162, v210, s68, v186\n"
    "v_perm_b32 v163, v210, s68, v187\n"
    "v_mfma_f32_32x32x16_bf16 v[128:143], v[176:179], a[148:151], v[128:143]\n"
    "buffer_load_dwordx4 v251, s[24:27], s55 offen offset:3072 lds\n"
    "s_add_u32 m0, s80, 0x8000\n"
    "v_add_u32 v251, 0x1000, v251\n"
    "v_perm_b32 v164, v210, s68, v188\n"
    "v_perm_b32 v165, v210, s68, v189\n"
    "v_perm_b32 v166, v210, s68, v190\n"
    "v_perm_b32 v167, v210, s68, v191\n"
    "v_cvt_pk_bf16_f32 v108, v108, v109\n"
    "v_mfma_f32_32x32x16_bf16 v[144:159], v[172:175], a[144:147], 0\n"
    "s_waitcnt vmcnt(12)\n"
    "ds_read_b64_tr_b16 a[124:125], v249 offset:8192\n"
    "ds_read_b64_tr_b16 a[126:127], v249 offset:8704\n"
    "buffer_load_dwordx4 v251, s[24:27], 0 offen offset:0 lds\n"
    "v_cvt_pk_bf16_f32 v110, v110, v111\n"
    "v_cvt_pk_bf16_f32 v124, v124, v125\n"
    "v_cvt_pk_bf16_f32 v126, v126, v127\n"
    "v_cvt_pk_bf16_f32 v0, v96, v97\n"
    "v_cvt_pk_bf16_f32 v1, v98, v99\n"
    "v_mfma_f32_32x32x16_bf16 v[144:159], v[180:183], a[148:151], v[144:159]\n"
    "ds_read_b64_tr_b16 a[132:133], v250 offset:8192\n"
    "ds_read_b64_tr_b16 a[134:135], v250 offset:8704\n"
    "buffer_load_dwordx4 v251, s[24:27], s53 offen offset:1024 lds\n"
    "v_cvt_pk_bf16_f32 v2, v112, v113\n"
    "v_cvt_pk_bf16_f32 v3, v114, v115\n"
    "v_cvt_pk_bf16_f32 v32, v100, v101\n"
    "v_cvt_pk_bf16_f32 v33, v102, v103\n"
    "v_mfma_f32_16x16x32_bf16 v[220:223], a[96:99], a[148:151], v[220:223]\n"
    "ds_read_b64_tr_b16 a[128:129], v249 offset:8448\n"
    "ds_read_b64_tr_b16 a[130:131], v249 offset:8960\n"
    "buffer_load_dwordx4 v251, s[24:27], s54 offen offset:2048 lds\n"
    "v_cvt_pk_bf16_f32 v34, v116, v117\n"
    "v_cvt_pk_bf16_f32 v35, v118, v119\n"
    "v_cvt_pk_bf16_f32 v64, v104, v105\n"
    "v_cvt_pk_bf16_f32 v65, v106, v107\n"
    "v_cvt_pk_bf16_f32 v66, v120, v121\n"
    "v_mfma_f32_16x16x32_bf16 v[216:219], a[96:99], a[152:155], v[216:219]\n"
    "ds_read_b64_tr_b16 a[136:137], v250 offset:9216\n"
    "ds_read_b64_tr_b16 a[138:139], v250 offset:9728\n"
    "buffer_load_dwordx4 v251, s[24:27], s55 offen offset:3072 lds\n"
    "s_add_u32 m0, s80, 0x9000\n"
    "v_add_u32 v251, 0x1000, v251\n"
    "v_cvt_pk_bf16_f32 v67, v122, v123\n"
    "v_accvgpr_write_b32 a0, v108\n"
    "v_accvgpr_write_b32 a1, v110\n"
    "v_accvgpr_write_b32 a2, v124\n"
    "v_accvgpr_write_b32 a3, v126\n"
    "v_pk_mul_f32 v[128:129], v[128:129], v[192:193] op_sel_hi:[1,0]\n"
    "v_pk_mul_f32 v[130:131], v[130:131], v[192:193] op_sel_hi:[1,0]\n"
    "v_pk_mul_f32 v[132:133], v[132:133], v[192:193] op_sel_hi:[1,0]\n"
    "v_pk_mul_f32 v[134:135], v[134:135], v[192:193] op_sel_hi:[1,0]\n"
    "v_pk_mul_f32 v[136:137], v[136:137], v[192:193] op_sel_hi:[1,0]\n"
    "v_pk_mul_f32 v[138:139], v[138:139], v[192:193] op_sel_hi:[1,0]\n"
    "v_pk_mul_f32 v[140:141], v[140:141], v[192:193] op_sel_hi:[1,0]\n"
    "v_pk_mul_f32 v[142:143], v[142:143], v[192:193] op_sel_hi:[1,0]\n"
    "v_pk_mul_f32 v[144:145], v[144:145], v[192:193] op_sel_hi:[1,0]\n"
    "v_pk_mul_f32 v[146:147], v[146:147], v[192:193] op_sel_hi:[1,0]\n"
    "v_pk_mul_f32 v[148:149], v[148:149], v[192:193] op_sel_hi:[1,0]\n"
    "v_pk_mul_f32 v[150:151], v[150:151], v[192:193] op_sel_hi:[1,0]\n"
    "v_pk_mul_f32 v[152:153], v[152:153], v[192:193] op_sel_hi:[1,0]\n"
    "v_pk_mul_f32 v[154:155], v[154:155], v[192:193] op_sel_hi:[1,0]\n"
    "v_pk_mul_f32 v[156:157], v[156:157], v[192:193] op_sel_hi:[1,0]\n"
    "v_pk_mul_f32 v[158:159], v[158:159], v[192:193] op_sel_hi:[1,0]\n"
    "s_barrier\n"
    "ds_read_b32 v242, v246 offset:0\n"
    "ds_read_b32 v243, v246 offset:40960\n"
    "ds_read_b32 v244, v247 offset:0\n"
    "ds_read_b32 v245, v247 offset:40960\n"
    "v_mov_b32 v201, 0\n"
    "ds_read_b32 v201, v201 offset:7616\n"
    "s_waitcnt lgkmcnt(5)\n"
    "v_mfma_f32_32x32x16_bf16 v[96:111], v[160:163], a[124:127], 0\n"
    "ds_read_b64 a[96:97], v252 offset:6240\n"
    "ds_read_b64 a[98:99], v252 offset:6256\n"
    "buffer_load_dwordx4 v251, s[24:27], 0 offen offset:0 lds\n"
    "v_perm_b32 v168, v211, s68, v184\n"
    "v_perm_b32 v169, v211, s68, v185\n"
    "v_perm_b32 v170, v211, s68, v186\n"
    "v_perm_b32 v171, v211, s68, v187\n"
    "v_mfma_f32_32x32x16_bf16 v[96:111], v[176:179], a[132:135], v[96:111]\n"
    "buffer_load_dwordx4 v251, s[24:27], s53 offen offset:1024 lds\n"
    "v_perm_b32 v172, v211, s68, v188\n"
    "v_perm_b32 v173, v211, s68, v189\n"
    "v_perm_b32 v174, v211, s68, v190\n"
    "v_perm_b32 v175, v211, s68, v191\n"
    "v_cvt_pk_bf16_f32 v140, v140, v141\n"
    "v_mfma_f32_32x32x16_bf16 v[112:127], v[164:167], a[128:131], 0\n"
    "s_waitcnt vmcnt(14)\n"
    "ds_read_b64_tr_b16 a[140:141], v249 offset:12288\n"
    "ds_read_b64_tr_b16 a[142:143], v249 offset:12800\n"
    "buffer_load_dwordx4 v251, s[24:27], s54 offen offset:2048 lds\n"
    "v_cvt_pk_bf16_f32 v142, v142, v143\n"
    "v_cvt_pk_bf16_f32 v156, v156, v157\n"
    "v_cvt_pk_bf16_f32 v158, v158, v159\n"
    "v_cvt_pk_bf16_f32 v4, v128, v129\n"
    "v_cvt_pk_bf16_f32 v5, v130, v131\n"
    "v_mfma_f32_32x32x16_bf16 v[112:127], v[180:183], a[132:135], v[112:127]\n"
    "ds_read_b64_tr_b16 a[148:149], v250 offset:12288\n"
    "ds_read_b64_tr_b16 a[150:151], v250 offset:12800\n"
    "buffer_load_dwordx4 v251, s[24:27], s55 offen offset:3072 lds\n"
    "v_cvt_pk_bf16_f32 v6, v144, v145\n"
    "v_cvt_pk_bf16_f32 v7, v146, v147\n"
    "v_cvt_pk_bf16_f32 v36, v132, v133\n"
    "v_cvt_pk_bf16_f32 v37, v134, v135\n"
    "v_mfma_f32_16x16x32_bf16 v[220:223], a[92:95], a[132:135], v[220:223]\n"
    "ds_read_b64_tr_b16 a[144:145], v249 offset:12544\n"
    "ds_read_b64_tr_b16 a[146:147], v249 offset:13056\n"
    "s_add_u32 m0, s80, 0x0\n"
    "s_waitcnt lgkmcnt(12)\n"
    "v_perm_b32 v246, v242, v241, s60\n"
    "buffer_load_dwordx4 v246, s[84:87], s48 offen offset:0 nt lds\n"
    "v_cvt_pk_bf16_f32 v38, v148, v149\n"
    "v_cvt_pk_bf16_f32 v39, v150, v151\n"
    "v_cvt_pk_bf16_f32 v68, v136, v137\n"
    "v_cvt_pk_bf16_f32 v69, v138, v139\n"
    "v_cvt_pk_bf16_f32 v70, v152, v153\n"
    "v_mfma_f32_16x16x32_bf16 v[216:219], a[92:95], a[136:139], v[216:219]\n"
    "ds_read_b64_tr_b16 a[152:153], v250 offset:13312\n"
    "ds_read_b64_tr_b16 a[154:155], v250 offset:13824\n"
    "buffer_load_dwordx4 v246, s[84:87], s49 offen offset:1024 nt lds\n"
    "v_cvt_pk_bf16_f32 v71, v154, v155\n"
    "v_accvgpr_write_b32 a4, v140\n"
    "v_accvgpr_write_b32 a5, v142\n"
    "v_accvgpr_write_b32 a6, v156\n"
    "v_accvgpr_write_b32 a7, v158\n"
    "v_pk_mul_f32 v[96:97], v[96:97], v[192:193] op_sel_hi:[1,0]\n"
    "v_pk_mul_f32 v[98:99], v[98:99], v[192:193] op_sel_hi:[1,0]\n"
    "v_pk_mul_f32 v[100:101], v[100:101], v[192:193] op_sel_hi:[1,0]\n"
    "v_pk_mul_f32 v[102:103], v[102:103], v[192:193] op_sel_hi:[1,0]\n"
    "v_pk_mul_f32 v[104:105], v[104:105], v[192:193] op_sel_hi:[1,0]\n"
    "v_pk_mul_f32 v[106:107], v[106:107], v[192:193] op_sel_hi:[1,0]\n"
    "v_pk_mul_f32 v[108:109], v[108:109], v[192:193] op_sel_hi:[1,0]\n"
    "v_pk_mul_f32 v[110:111], v[110:111], v[192:193] op_sel_hi:[1,0]\n"
    "v_pk_mul_f32 v[112:113], v[112:113], v[192:193] op_sel_hi:[1,0]\n"
    "v_pk_mul_f32 v[114:115], v[114:115], v[192:193] op_sel_hi:[1,0]\n"
    "v_pk_mul_f32 v[116:117], v[116:117], v[192:193] op_sel_hi:[1,0]\n"
    "v_pk_mul_f32 v[118:119], v[118:119], v[192:193] op_sel_hi:[1,0]\n"
    "v_pk_mul_f32 v[120:121], v[120:121], v[192:193] op_sel_hi:[1,0]\n"
    "v_pk_mul_f32 v[122:123], v[122:123], v[192:193] op_sel_hi:[1,0]\n"
    "v_pk_mul_f32 v[124:125], v[124:125], v[192:193] op_sel_hi:[1,0]\n"
    "v_pk_mul_f32 v[126:127], v[126:127], v[192:193] op_sel_hi:[1,0]\n"
    "s_waitcnt lgkmcnt(0)\n"
    "v_mfma_f32_32x32x16_bf16 v[128:143], v[168:171], a[140:143], 0\n"
    "ds_read_b64 a[92:93], v252 offset:6272\n"
    "ds_read_b64 a[94:95], v252 offset:6288\n"
    "v_perm_b32 v160, v212, s68, v184\n"
    "v_perm_b32 v161, v212, s68, v185\n"
    "v_perm_b32 v162, v212, s68, v186\n"
    "v_perm_b32 v163, v212, s68, v187\n"
    "v_mfma_f32_32x32x16_bf16 v[128:143], v[176:179], a[148:151], v[128:143]\n"
    "v_perm_b32 v164, v212, s68, v188\n"
    "v_perm_b32 v165, v212, s68, v189\n"
    "v_perm_b32 v166, v212, s68, v190\n"
    "v_perm_b32 v167, v212, s68, v191\n"
    "v_cvt_pk_bf16_f32 v108, v108, v109\n"
    "v_mfma_f32_32x32x16_bf16 v[144:159], v[172:175], a[144:147], 0\n"
    "s_waitcnt vmcnt(14)\n"
    "ds_read_b64_tr_b16 a[124:125], v249 offset:16384\n"
    "ds_read_b64_tr_b16 a[126:127], v249 offset:16896\n"
    "v_perm_b32 v247, v243, v241, s60\n"
    "buffer_load_dwordx4 v247, s[88:91], s50 offen offset:2048 nt lds\n"
    "v_cvt_pk_bf16_f32 v110, v110, v111\n"
    "v_cvt_pk_bf16_f32 v124, v124, v125\n"
    "v_cvt_pk_bf16_f32 v126, v126, v127\n"
    "v_cvt_pk_bf16_f32 v8, v96, v97\n"
    "v_cvt_pk_bf16_f32 v9, v98, v99\n"
    "v_mfma_f32_32x32x16_bf16 v[144:159], v[180:183], a[148:151], v[144:159]\n"
    "ds_read_b64_tr_b16 a[132:133], v250 offset:16384\n"
    "ds_read_b64_tr_b16 a[134:135], v250 offset:16896\n"
    "v_cvt_pk_bf16_f32 v10, v112, v113\n"
    "v_cvt_pk_bf16_f32 v11, v114, v115\n"
    "v_cvt_pk_bf16_f32 v40, v100, v101\n"
    "v_cvt_pk_bf16_f32 v41, v102, v103\n"
    "v_mfma_f32_16x16x32_bf16 v[220:223], a[96:99], a[148:151], v[220:223]\n"
    "ds_read_b64_tr_b16 a[128:129], v249 offset:16640\n"
    "ds_read_b64_tr_b16 a[130:131], v249 offset:17152\n"
    "v_cvt_pk_bf16_f32 v42, v116, v117\n"
    "v_cvt_pk_bf16_f32 v43, v118, v119\n"
    "v_cvt_pk_bf16_f32 v72, v104, v105\n"
    "v_cvt_pk_bf16_f32 v73, v106, v107\n"
    "v_cvt_pk_bf16_f32 v74, v120, v121\n"
    "v_mfma_f32_16x16x32_bf16 v[216:219], a[96:99], a[152:155], v[216:219]\n"
    "ds_read_b64_tr_b16 a[136:137], v250 offset:17408\n"
    "ds_read_b64_tr_b16 a[138:139], v250 offset:17920\n"
    "buffer_load_dwordx4 v247, s[88:91], s51 offen offset:3072 nt lds\n"
    "v_cvt_pk_bf16_f32 v75, v122, v123\n"
    "v_accvgpr_write_b32 a8, v108\n"
    "v_accvgpr_write_b32 a9, v110\n"
    "v_accvgpr_write_b32 a10, v124\n"
    "v_accvgpr_write_b32 a11, v126\n"
    "v_pk_mul_f32 v[128:129], v[128:129], v[192:193] op_sel_hi:[1,0]\n"
    "v_pk_mul_f32 v[130:131], v[130:131], v[192:193] op_sel_hi:[1,0]\n"
    "v_pk_mul_f32 v[132:133], v[132:133], v[192:193] op_sel_hi:[1,0]\n"
    "v_pk_mul_f32 v[134:135], v[134:135], v[192:193] op_sel_hi:[1,0]\n"
    "v_pk_mul_f32 v[136:137], v[136:137], v[192:193] op_sel_hi:[1,0]\n"
    "v_pk_mul_f32 v[138:139], v[138:139], v[192:193] op_sel_hi:[1,0]\n"
    "v_pk_mul_f32 v[140:141], v[140:141], v[192:193] op_sel_hi:[1,0]\n"
    "v_pk_mul_f32 v[142:143], v[142:143], v[192:193] op_sel_hi:[1,0]\n"
    "v_pk_mul_f32 v[144:145], v[144:145], v[192:193] op_sel_hi:[1,0]\n"
    "v_pk_mul_f32 v[146:147], v[146:147], v[192:193] op_sel_hi:[1,0]\n"
    "v_pk_mul_f32 v[148:149], v[148:149], v[192:193] op_sel_hi:[1,0]\n"
    "v_pk_mul_f32 v[150:151], v[150:151], v[192:193] op_sel_hi:[1,0]\n"
    "v_pk_mul_f32 v[152:153], v[152:153], v[192:193] op_sel_hi:[1,0]\n"
    "v_pk_mul_f32 v[154:155], v[154:155], v[192:193] op_sel_hi:[1,0]\n"
    "v_pk_mul_f32 v[156:157], v[156:157], v[192:193] op_sel_hi:[1,0]\n"
    "v_pk_mul_f32 v[158:159], v[158:159], v[192:193] op_sel_hi:[1,0]\n"
    "s_waitcnt lgkmcnt(0)\n"
    "v_mfma_f32_32x32x16_bf16 v[96:111], v[160:163], a[124:127], 0\n"
    "ds_read_b64 a[96:97], v252 offset:6304\n"
    "ds_read_b64 a[98:99], v252 offset:6320\n"
    "v_perm_b32 v168, v213, s68, v184\n"
    "v_perm_b32 v169, v213, s68, v185\n"
    "v_perm_b32 v170, v213, s68, v186\n"
    "v_perm_b32 v171, v213, s68, v187\n"
    "v_mfma_f32_32x32x16_bf16 v[96:111], v[176:179], a[132:135], v[96:111]\n"
    "v_perm_b32 v172, v213, s68, v188\n"
    "v_perm_b32 v173, v213, s68, v189\n"
    "v_perm_b32 v174, v213, s68, v190\n"
    "v_perm_b32 v175, v213, s68, v191\n"
    "v_cvt_pk_bf16_f32 v140, v140, v141\n"
    "v_mfma_f32_32x32x16_bf16 v[112:127], v[164:167], a[128:131], 0\n"
    "s_waitcnt vmcnt(12)\n"
    "ds_read_b64_tr_b16 a[140:141], v249 offset:20480\n"
    "ds_read_b64_tr_b16 a[142:143], v249 offset:20992\n"
    "s_add_u32 m0, s80, 0x1000\n"
    "v_perm_b32 v246, v244, v241, s60\n"
    "buffer_load_dwordx4 v246, s[92:95], s48 offen offset:0 nt lds\n"
    "v_cvt_pk_bf16_f32 v142, v142, v143\n"
    "v_cvt_pk_bf16_f32 v156, v156, v157\n"
    "v_cvt_pk_bf16_f32 v158, v158, v159\n"
    "v_cvt_pk_bf16_f32 v12, v128, v129\n"
    "v_cvt_pk_bf16_f32 v13, v130, v131\n"
    "v_mfma_f32_32x32x16_bf16 v[112:127], v[180:183], a[132:135], v[112:127]\n"
    "ds_read_b64_tr_b16 a[148:149], v250 offset:20480\n"
    "ds_read_b64_tr_b16 a[150:151], v250 offset:20992\n"
    "v_cvt_pk_bf16_f32 v14, v144, v145\n"
    "v_cvt_pk_bf16_f32 v15, v146, v147\n"
    "v_cvt_pk_bf16_f32 v44, v132, v133\n"
    "v_cvt_pk_bf16_f32 v45, v134, v135\n"
    "v_mfma_f32_16x16x32_bf16 v[220:223], a[92:95], a[132:135], v[220:223]\n"
    "ds_read_b64_tr_b16 a[144:145], v249 offset:20736\n"
    "ds_read_b64_tr_b16 a[146:147], v249 offset:21248\n"
    "v_cvt_pk_bf16_f32 v46, v148, v149\n"
    "v_cvt_pk_bf16_f32 v47, v150, v151\n"
    "v_cvt_pk_bf16_f32 v76, v136, v137\n"
    "v_cvt_pk_bf16_f32 v77, v138, v139\n"
    "v_cvt_pk_bf16_f32 v78, v152, v153\n"
    "v_mfma_f32_16x16x32_bf16 v[216:219], a[92:95], a[136:139], v[216:219]\n"
    "ds_read_b64_tr_b16 a[152:153], v250 offset:21504\n"
    "ds_read_b64_tr_b16 a[154:155], v250 offset:22016\n"
    "buffer_load_dwordx4 v246, s[92:95], s49 offen offset:1024 nt lds\n"
    "v_cvt_pk_bf16_f32 v79, v154, v155\n"
    "v_accvgpr_write_b32 a12, v140\n"
    "v_accvgpr_write_b32 a13, v142\n"
    "v_accvgpr_write_b32 a14, v156\n"
    "v_accvgpr_write_b32 a15, v158\n"
    "v_pk_mul_f32 v[96:97], v[96:97], v[192:193] op_sel_hi:[1,0]\n"
    "v_pk_mul_f32 v[98:99], v[98:99], v[192:193] op_sel_hi:[1,0]\n"
    "v_pk_mul_f32 v[100:101], v[100:101], v[192:193] op_sel_hi:[1,0]\n"
    "v_pk_mul_f32 v[102:103], v[102:103], v[192:193] op_sel_hi:[1,0]\n"
    "v_pk_mul_f32 v[104:105], v[104:105], v[192:193] op_sel_hi:[1,0]\n"
    "v_pk_mul_f32 v[106:107], v[106:107], v[192:193] op_sel_hi:[1,0]\n"
    "v_pk_mul_f32 v[108:109], v[108:109], v[192:193] op_sel_hi:[1,0]\n"
    "v_pk_mul_f32 v[110:111], v[110:111], v[192:193] op_sel_hi:[1,0]\n"
    "v_pk_mul_f32 v[112:113], v[112:113], v[192:193] op_sel_hi:[1,0]\n"
    "v_pk_mul_f32 v[114:115], v[114:115], v[192:193] op_sel_hi:[1,0]\n"
    "v_pk_mul_f32 v[116:117], v[116:117], v[192:193] op_sel_hi:[1,0]\n"
    "v_pk_mul_f32 v[118:119], v[118:119], v[192:193] op_sel_hi:[1,0]\n"
    "v_pk_mul_f32 v[120:121], v[120:121], v[192:193] op_sel_hi:[1,0]\n"
    "v_pk_mul_f32 v[122:123], v[122:123], v[192:193] op_sel_hi:[1,0]\n"
    "v_pk_mul_f32 v[124:125], v[124:125], v[192:193] op_sel_hi:[1,0]\n"
    "v_pk_mul_f32 v[126:127], v[126:127], v[192:193] op_sel_hi:[1,0]\n"
    "s_waitcnt lgkmcnt(0)\n"
    "v_mfma_f32_32x32x16_bf16 v[128:143], v[168:171], a[140:143], 0\n"
    "ds_read_b64 a[92:93], v252 offset:6336\n"
    "ds_read_b64 a[94:95], v252 offset:6352\n"
    "v_readfirstlane_b32 s35, v201\n"
    "v_fma_f32 v196, v202, v192, v196\n"
    "v_fma_f32 v198, v203, s57, v198\n"
    "v_mov_b32 v197, v196\n"
    "v_mul_f32 v198, s65, v198\n"
    "s_nop 0\n"
    "v_mfma_f32_32x32x16_bf16 v[128:143], v[176:179], a[148:151], v[128:143]\n"
    "v_permlane16_swap_b32 v196, v197\n"
    "s_cmp_lg_u32 s35, 0\n"
    "s_cbranch_scc1 L_pf_%=\n"
    "L_nopf_%=:\n"
    "v_perm_b32 v160, v214, s68, v184\n"
    "v_perm_b32 v161, v214, s68, v185\n"
    "v_perm_b32 v162, v214, s68, v186\n"
    "v_perm_b32 v163, v214, s68, v187\n"
    "v_mfma_f32_32x32x16_bf16 v[144:159], v[172:175], a[144:147], 0\n"
    "s_waitcnt vmcnt(10)\n"
    "ds_read_b64_tr_b16 a[124:125], v249 offset:24576\n"
    "ds_read_b64_tr_b16 a[126:127], v249 offset:25088\n"
    "s_add_u32 m0, s80, 0x2000\n"
    "v_perm_b32 v247, v245, v241, s60\n"
    "buffer_load_dwordx4 v247, s[96:99], s48 offen offset:0 nt lds\n"
    "v_perm_b32 v164, v214, s68, v188\n"
    "v_perm_b32 v165, v214, s68, v189\n"
    "v_perm_b32 v166, v214, s68, v190\n"
    "v_perm_b32 v167, v214, s68, v191\n"
    "v_cvt_pk_bf16_f32 v108, v108, v109\n"
    "v_cvt_pk_bf16_f32 v110, v110, v111\n"
    "v_mfma_f32_32x32x16_bf16 v[144:159], v[180:183], a[148:151], v[144:159]\n"
    "ds_read_b64_tr_b16 a[132:133], v250 offset:24576\n"
    "ds_read_b64_tr_b16 a[134:135], v250 offset:25088\n"
    "v_cvt_pk_bf16_f32 v124, v124, v125\n"
    "v_cvt_pk_bf16_f32 v126, v126, v127\n"
    "v_cvt_pk_bf16_f32 v16, v96, v97\n"
    "v_cvt_pk_bf16_f32 v17, v98, v99\n"
    "v_cvt_pk_bf16_f32 v18, v112, v113\n"
    "v_cvt_pk_bf16_f32 v19, v114, v115\n"
    "v_mfma_f32_16x16x32_bf16 v[220:223], a[96:99], a[148:151], v[220:223]\n"
    "ds_read_b64_tr_b16 a[128:129], v249 offset:24832\n"
    "ds_read_b64_tr_b16 a[130:131], v249 offset:25344\n"
    "v_cvt_pk_bf16_f32 v48, v100, v101\n"
    "v_cvt_pk_bf16_f32 v49, v102, v103\n"
    "v_cvt_pk_bf16_f32 v50, v116, v117\n"
    "v_cvt_pk_bf16_f32 v51, v118, v119\n"
    "v_cvt_pk_bf16_f32 v80, v104, v105\n"
    "v_cvt_pk_bf16_f32 v81, v106, v107\n"
    "v_mfma_f32_16x16x32_bf16 v[216:219], a[96:99], a[152:155], v[216:219]\n"
    "ds_read_b64_tr_b16 a[136:137], v250 offset:25600\n"
    "ds_read_b64_tr_b16 a[138:139], v250 offset:26112\n"
    "buffer_load_dwordx4 v247, s[96:99], s49 offen offset:1024 nt lds\n"
    "v_cvt_pk_bf16_f32 v82, v120, v121\n"
    "v_cvt_pk_bf16_f32 v83, v122, v123\n"
    "v_accvgpr_write_b32 a16, v108\n"
    "v_accvgpr_write_b32 a17, v110\n"
    "v_accvgpr_write_b32 a18, v124\n"
    "v_accvgpr_write_b32 a19, v126\n"
    "v_pk_mul_f32 v[128:129], v[128:129], v[192:193] op_sel_hi:[1,0]\n"
    "v_pk_mul_f32 v[130:131], v[130:131], v[192:193] op_sel_hi:[1,0]\n"
    "v_pk_mul_f32 v[132:133], v[132:133], v[192:193] op_sel_hi:[1,0]\n"
    "v_pk_mul_f32 v[134:135], v[134:135], v[192:193] op_sel_hi:[1,0]\n"
    "v_pk_mul_f32 v[136:137], v[136:137], v[192:193] op_sel_hi:[1,0]\n"
    "v_pk_mul_f32 v[138:139], v[138:139], v[192:193] op_sel_hi:[1,0]\n"
    "v_pk_mul_f32 v[140:141], v[140:141], v[192:193] op_sel_hi:[1,0]\n"
    "v_pk_mul_f32 v[142:143], v[142:143], v[192:193] op_sel_hi:[1,0]\n"
    "v_pk_mul_f32 v[144:145], v[144:145], v[192:193] op_sel_hi:[1,0]\n"
    "v_pk_mul_f32 v[146:147], v[146:147], v[192:193] op_sel_hi:[1,0]\n"
    "v_pk_mul_f32 v[148:149], v[148:149], v[192:193] op_sel_hi:[1,0]\n"
    "v_pk_mul_f32 v[150:151], v[150:151], v[192:193] op_sel_hi:[1,0]\n"
    "v_pk_mul_f32 v[152:153], v[152:153], v[192:193] op_sel_hi:[1,0]\n"
    "v_pk_mul_f32 v[154:155], v[154:155], v[192:193] op_sel_hi:[1,0]\n"
    "v_pk_mul_f32 v[156:157], v[156:157], v[192:193] op_sel_hi:[1,0]\n"
    "v_pk_mul_f32 v[158:159], v[158:159], v[192:193] op_sel_hi:[1,0]\n"
    "s_waitcnt lgkmcnt(0)\n"
    "v_mfma_f32_32x32x16_bf16 v[96:111], v[160:163], a[124:127], 0\n"
    "ds_read_b64 a[96:97], v252 offset:6368\n"
    "ds_read_b64 a[98:99], v252 offset:6384\n"
    "v_perm_b32 v168, v215, s68, v184\n"
    "v_perm_b32 v169, v215, s68, v185\n"
    "v_perm_b32 v170, v215, s68, v186\n"
    "v_perm_b32 v171, v215, s68, v187\n"
    "v_mfma_f32_32x32x16_bf16 v[96:111], v[176:179], a[132:135], v[96:111]\n"
    "v_perm_b32 v172, v215, s68, v188\n"
    "v_perm_b32 v173, v215, s68, v189\n"
    "v_perm_b32 v174, v215, s68, v190\n"
    "v_perm_b32 v175, v215, s68, v191\n"
    "v_cvt_pk_bf16_f32 v140, v140, v141\n"
    "v_mfma_f32_32x32x16_bf16 v[112:127], v[164:167], a[128:131], 0\n"
    "s_waitcnt vmcnt(8)\n"
    "ds_read_b64_tr_b16 a[140:141], v249 offset:28672\n"
    "ds_read_b64_tr_b16 a[142:143], v249 offset:29184\n"
    "v_perm_b32 v246, v242, v241, s63\n"
    "buffer_load_dwordx4 v246, s[84:87], s50 offen offset:2048 nt lds\n"
    "v_cvt_pk_bf16_f32 v142, v142, v143\n"
    "v_cvt_pk_bf16_f32 v156, v156, v157\n"
    "v_cvt_pk_bf16_f32 v158, v158, v159\n"
    "v_cvt_pk_bf16_f32 v20, v128, v129\n"
    "v_cvt_pk_bf16_f32 v21, v130, v131\n"
    "v_mfma_f32_32x32x16_bf16 v[112:127], v[180:183], a[132:135], v[112:127]\n"
    "ds_read_b64_tr_b16 a[148:149], v250 offset:28672\n"
    "ds_read_b64_tr_b16 a[150:151], v250 offset:29184\n"
    "v_cvt_pk_bf16_f32 v22, v144, v145\n"
    "v_cvt_pk_bf16_f32 v23, v146, v147\n"
    "v_cvt_pk_bf16_f32 v52, v132, v133\n"
    "v_cvt_pk_bf16_f32 v53, v134, v135\n"
    "v_mfma_f32_16x16x32_bf16 v[220:223], a[92:95], a[132:135], v[220:223]\n"
    "ds_read_b64_tr_b16 a[144:145], v249 offset:28928\n"
    "ds_read_b64_tr_b16 a[146:147], v249 offset:29440\n"
    "v_cvt_pk_bf16_f32 v54, v148, v149\n"
    "v_cvt_pk_bf16_f32 v55, v150, v151\n"
    "v_cvt_pk_bf16_f32 v84, v136, v137\n"
    "v_cvt_pk_bf16_f32 v85, v138, v139\n"
    "v_cvt_pk_bf16_f32 v86, v152, v153\n"
    "v_mfma_f32_16x16x32_bf16 v[216:219], a[92:95], a[136:139], v[216:219]\n"
    "ds_read_b64_tr_b16 a[152:153], v250 offset:29696\n"
    "ds_read_b64_tr_b16 a[154:155], v250 offset:30208\n"
    "buffer_load_dwordx4 v246, s[84:87], s51 offen offset:3072 nt lds\n"
    "v_cvt_pk_bf16_f32 v87, v154, v155\n"
    "v_accvgpr_write_b32 a20, v140\n"
    "v_accvgpr_write_b32 a21, v142\n"
    "v_accvgpr_write_b32 a22, v156\n"
    "v_accvgpr_write_b32 a23, v158\n"
    "v_pk_mul_f32 v[96:97], v[96:97], v[192:193] op_sel_hi:[1,0]\n"
    "v_pk_mul_f32 v[98:99], v[98:99], v[192:193] op_sel_hi:[1,0]\n"
    "v_pk_mul_f32 v[100:101], v[100:101], v[192:193] op_sel_hi:[1,0]\n"
    "v_pk_mul_f32 v[102:103], v[102:103], v[192:193] op_sel_hi:[1,0]\n"
    "v_pk_mul_f32 v[104:105], v[104:105], v[192:193] op_sel_hi:[1,0]\n"
    "v_pk_mul_f32 v[106:107], v[106:107], v[192:193] op_sel_hi:[1,0]\n"
    "v_pk_mul_f32 v[108:109], v[108:109], v[192:193] op_sel_hi:[1,0]\n"
    "v_pk_mul_f32 v[110:111], v[110:111], v[192:193] op_sel_hi:[1,0]\n"
    "v_pk_mul_f32 v[112:113], v[112:113], v[192:193] op_sel_hi:[1,0]\n"
    "v_pk_mul_f32 v[114:115], v[114:115], v[192:193] op_sel_hi:[1,0]\n"
    "v_pk_mul_f32 v[116:117], v[116:117], v[192:193] op_sel_hi:[1,0]\n"
    "v_pk_mul_f32 v[118:119], v[118:119], v[192:193] op_sel_hi:[1,0]\n"
    "v_pk_mul_f32 v[120:121], v[120:121], v[192:193] op_sel_hi:[1,0]\n"
    "v_pk_mul_f32 v[122:123], v[122:123], v[192:193] op_sel_hi:[1,0]\n"
    "v_pk_mul_f32 v[124:125], v[124:125], v[192:193] op_sel_hi:[1,0]\n"
    "v_pk_mul_f32 v[126:127], v[126:127], v[192:193] op_sel_hi:[1,0]\n"
    "s_waitcnt lgkmcnt(0)\n"
    "v_mfma_f32_16x16x32_bf16 v[220:223], a[96:99], a[148:151], v[220:223]\n"
    "v_cvt_pk_bf16_f32 v108, v108, v109\n"
    "v_cvt_pk_bf16_f32 v110, v110, v111\n"
    "v_mfma_f32_16x16x32_bf16 v[216:219], a[96:99], a[152:155], v[216:219]\n"
    "v_cvt_pk_bf16_f32 v124, v124, v125\n"
    "v_cvt_pk_bf16_f32 v126, v126, v127\n"
    "v_mfma_f32_32x32x16_bf16 v[128:143], v[168:171], a[140:143], 0\n"
    "s_waitcnt vmcnt(8)\n"
    "s_barrier\n"
    "ds_read_b128 a[32:35], v226 offset:0\n"
    "ds_read_b128 a[36:39], v227 offset:0\n"
    "v_cvt_pk_bf16_f32 v24, v96, v97\n"
    "v_cvt_pk_bf16_f32 v25, v98, v99\n"
    "v_cvt_pk_bf16_f32 v26, v112, v113\n"
    "v_cvt_pk_bf16_f32 v27, v114, v115\n"
    "v_mfma_f32_32x32x16_bf16 v[128:143], v[176:179], a[148:151], v[128:143]\n"
    "ds_read_b128 a[40:43], v226 offset:512\n"
    "ds_read_b128 a[44:47], v227 offset:512\n"
    "v_sub_f32 v216, v216, v220\n"
    "v_sub_f32 v217, v217, v221\n"
    "v_sub_f32 v218, v218, v222\n"
    "v_sub_f32 v219, v219, v223\n"
    "v_cvt_pk_bf16_f32 v56, v100, v101\n"
    "v_mfma_f32_32x32x16_bf16 v[144:159], v[172:175], a[144:147], 0\n"
    "ds_read_b128 a[48:51], v226 offset:1024\n"
    "ds_read_b128 a[52:55], v227 offset:1024\n"
    "v_fma_f32 v216, v216, v194, v196\n"
    "v_fma_f32 v217, v217, v195, v197\n"
    "v_fma_f32 v218, v218, v194, v196\n"
    "v_fma_f32 v219, v219, v195, v197\n"
    "v_mfma_f32_32x32x16_bf16 v[144:159], v[180:183], a[148:151], v[144:159]\n"
    "ds_write_b32 v238, v216 offset:3072\n"
    "ds_write_b32 v238, v217 offset:3136\n"
    "v_cvt_pk_bf16_f32 v57, v102, v103\n"
    "v_cvt_pk_bf16_f32 v58, v116, v117\n"
    "v_cvt_pk_bf16_f32 v59, v118, v119\n"
    "v_cvt_pk_bf16_f32 v88, v104, v105\n"
    "v_cvt_pk_bf16_f32 v89, v106, v107\n"
    "ds_write_b32 v238, v218 offset:3200\n"
    "ds_write_b32 v238, v219 offset:3264\n"
    "v_cvt_pk_bf16_f32 v90, v120, v121\n"
    "v_cvt_pk_bf16_f32 v91, v122, v123\n"
    "v_accvgpr_write_b32 a24, v108\n"
    "v_accvgpr_write_b32 a25, v110\n"
    "v_accvgpr_write_b32 a26, v124\n"
    "v_accvgpr_write_b32 a27, v126\n"
    "ds_read_b128 v[96:99], v239 offset:3072\n"
    "ds_read_b128 v[100:103], v239 offset:3104\n"
    "ds_read_b128 v[104:107], v239 offset:3136\n"
    "ds_read_b128 v[108:111], v239 offset:3168\n"
    "ds_read_b128 a[56:59], v226 offset:1536\n"
    "ds_read_b128 a[60:63], v227 offset:1536\n"
    "s_add_u32 m0, s80, 0x3000\n"
    "v_perm_b32 v247, v243, v241, s63\n"
    "buffer_load_dwordx4 v247, s[88:91], s48 offen offset:0 nt lds\n"
    "buffer_load_dwordx4 v247, s[88:91], s49 offen offset:1024 nt lds\n"
    "v_pk_mul_f32 v[128:129], v[128:129], v[192:193] op_sel_hi:[1,0]\n"
    "v_pk_mul_f32 v[130:131], v[130:131], v[192:193] op_sel_hi:[1,0]\n"
    "v_pk_mul_f32 v[132:133], v[132:133], v[192:193] op_sel_hi:[1,0]\n"
    "v_pk_mul_f32 v[134:135], v[134:135], v[192:193] op_sel_hi:[1,0]\n"
    "v_pk_mul_f32 v[136:137], v[136:137], v[192:193] op_sel_hi:[1,0]\n"
    "v_pk_mul_f32 v[138:139], v[138:139], v[192:193] op_sel_hi:[1,0]\n"
    "v_pk_mul_f32 v[140:141], v[140:141], v[192:193] op_sel_hi:[1,0]\n"
    "v_pk_mul_f32 v[142:143], v[142:143], v[192:193] op_sel_hi:[1,0]\n"
    "v_pk_mul_f32 v[144:145], v[144:145], v[192:193] op_sel_hi:[1,0]\n"
    "v_pk_mul_f32 v[146:147], v[146:147], v[192:193] op_sel_hi:[1,0]\n"
    "v_pk_mul_f32 v[148:149], v[148:149], v[192:193] op_sel_hi:[1,0]\n"
    "v_pk_mul_f32 v[150:151], v[150:151], v[192:193] op_sel_hi:[1,0]\n"
    "v_pk_mul_f32 v[152:153], v[152:153], v[192:193] op_sel_hi:[1,0]\n"
    "v_pk_mul_f32 v[154:155], v[154:155], v[192:193] op_sel_hi:[1,0]\n"
    "v_pk_mul_f32 v[156:157], v[156:157], v[192:193] op_sel_hi:[1,0]\n"
    "v_pk_mul_f32 v[158:159], v[158:159], v[192:193] op_sel_hi:[1,0]\n"
    "s_and_b32 s46, s35, 0x05050505\n"
    "s_cbranch_scc1 L_gen_%=\n"
    "s_waitcnt vmcnt(8)\n"
    "s_waitcnt lgkmcnt(2)\n"
    "v_mfma_f32_32x32x16_bf16 v[160:175], v[0:3], a[32:35], v[96:111]\n"
    "s_barrier\n"
    "ds_read_b128 a[32:35], v226 offset:2048\n"
    "v_mov_b32 v223, 0\n"
    "v_mov_b32 v222, v198\n"
    "v_cvt_pk_bf16_f32 v28, v128, v129\n"
    "v_cvt_pk_bf16_f32 v29, v130, v131\n"
    "v_mfma_f32_32x32x16_bf16 v[160:175], v[4:7], a[36:39], v[160:175]\n"
    "ds_read_b128 a[36:39], v227 offset:2048\n"
    "ds_read_b128 v[112:115], v239 offset:3200\n"
    "v_cvt_pk_bf16_f32 v30, v144, v145\n"
    "v_cvt_pk_bf16_f32 v31, v146, v147\n"
    "v_cvt_pk_bf16_f32 v60, v132, v133\n"
    "v_cvt_pk_bf16_f32 v61, v134, v135\n"
    "v_mfma_f32_32x32x16_bf16 v[160:175], v[8:11], a[40:43], v[160:175]\n"
    "ds_read_b128 a[40:43], v226 offset:2560\n"
    "ds_read_b128 v[116:119], v239 offset:3232\n"
    "v_cvt_pk_bf16_f32 v62, v148, v149\n"
    "v_cvt_pk_bf16_f32 v63, v150, v151\n"
    "v_cvt_pk_bf16_f32 v92, v136, v137\n"
    "v_cvt_pk_bf16_f32 v93, v138, v139\n"
    "v_mfma_f32_32x32x16_bf16 v[160:175], v[12:15], a[44:47], v[160:175]\n"
    "ds_read_b128 a[44:47], v227 offset:2560\n"
    "ds_read_b128 v[120:123], v239 offset:3264\n"
    "v_cvt_pk_bf16_f32 v94, v152, v153\n"
    "v_cvt_pk_bf16_f32 v95, v154, v155\n"
    "v_cvt_pk_bf16_f32 v140, v140, v141\n"
    "v_cvt_pk_bf16_f32 v142, v142, v143\n"
    "v_mfma_f32_32x32x16_bf16 v[160:175], v[16:19], a[48:51], v[160:175]\n"
    "ds_read_b128 a[48:51], v226 offset:3072\n"
    "ds_read_b128 v[124:127], v239 offset:3296\n"
    "v_cvt_pk_bf16_f32 v156, v156, v157\n"
    "v_cvt_pk_bf16_f32 v158, v158, v159\n"
    "v_accvgpr_write_b32 a28, v140\n"
    "v_accvgpr_write_b32 a29, v142\n"
    "v_mfma_f32_32x32x16_bf16 v[160:175], v[20:23], a[52:55], v[160:175]\n"
    "ds_read_b128 a[52:55], v227 offset:3072\n"
    "ds_read_b128 v[192:195], v255 offset:7488\n"
    "v_accvgpr_write_b32 a30, v156\n"
    "v_accvgpr_write_b32 a31, v158\n"
    "s_waitcnt lgkmcnt(12)\n"
    "v_mfma_f32_32x32x16_bf16 v[160:175], v[24:27], a[56:59], v[160:175]\n"
    "ds_read_b128 a[56:59], v226 offset:3584\n"
    "ds_read_b128 v[196:199], v255 offset:7520\n"
    "s_waitcnt lgkmcnt(13)\n"
    "v_mfma_f32_32x32x16_bf16 v[160:175], v[28:31], a[60:63], v[160:175]\n"
    "ds_read_b128 a[60:63], v227 offset:3584\n"
    "ds_read_b128 v[200:203], v255 offset:7552\n"
    "ds_read_b128 v[204:207], v255 offset:7584\n"
    "ds_write_b32 v253, v223 offset:2176\n"
    "s_waitcnt lgkmcnt(8)\n"
    "v_mfma_f32_32x32x16_bf16 v[176:191], v[32:35], a[32:35], v[112:127]\n"
    "s_waitcnt vmcnt(6) lgkmcnt(0)\n"
    "s_barrier\n"
    "ds_read_b128 a[32:35], v226 offset:4096\n"
    "s_nop 2\n"
    "v_exp_f32_e64 v160, v160 clamp\n"
    "v_mfma_f32_32x32x16_bf16 v[176:191], v[36:39], a[36:39], v[176:191]\n"
    "ds_read_b128 a[36:39], v227 offset:4096\n"
    "ds_read_b128 v[128:131], v239 offset:3328\n"
    "v_exp_f32_e64 v161, v161 clamp\n"
    "v_exp_f32_e64 v162, v162 clamp\n"
    "v_mfma_f32_32x32x16_bf16 v[176:191], v[40:43], a[40:43], v[176:191]\n"
    "ds_read_b128 a[40:43], v226 offset:4608\n"
    "ds_read_b128 v[132:135], v239 offset:3360\n"
    "v_exp_f32_e64 v163, v163 clamp\n"
    "v_exp_f32_e64 v164, v164 clamp\n"
    "v_mfma_f32_32x32x16_bf16 v[176:191], v[44:47], a[44:47], v[176:191]\n"
    "ds_read_b128 a[44:47], v227 offset:4608\n"
    "ds_read_b128 v[136:139], v239 offset:3392\n"
    "v_exp_f32_e64 v165, v165 clamp\n"
    "v_exp_f32_e64 v166, v166 clamp\n"
    "v_mfma_f32_32x32x16_bf16 v[176:191], v[48:51], a[48:51], v[176:191]\n"
    "ds_read_b128 a[48:51], v226 offset:5120\n"
    "ds_read_b128 v[140:143], v239 offset:3424\n"
    "v_perm_b32 v246, v244, v241, s63\n"
    "v_perm_b32 v247, v245, v241, s63\n"
    "v_exp_f32_e64 v167, v167 clamp\n"
    "v_exp_f32_e64 v168, v168 clamp\n"
    "v_mfma_f32_32x32x16_bf16 v[176:191], v[52:55], a[52:55], v[176:191]\n"
    "ds_read_b128 a[52:55], v227 offset:5120\n"
    "v_exp_f32_e64 v169, v169 clamp\n"
    "v_exp_f32_e64 v170, v170 clamp\n"
    "v_mfma_f32_32x32x16_bf16 v[176:191], v[56:59], a[56:59], v[176:191]\n"
    "buffer_load_dwordx4 v246, s[92:95], s50 offen offset:2048 nt lds\n"
    "buffer_load_dwordx4 v246, s[92:95], s51 offen offset:3072 nt lds\n"
    "ds_read_b128 a[56:59], v226 offset:5632\n"
    "v_exp_f32_e64 v171, v171 clamp\n"
    "v_exp_f32_e64 v172, v172 clamp\n"
    "v_mfma_f32_32x32x16_bf16 v[176:191], v[60:63], a[60:63], v[176:191]\n"
    "s_add_u32 m0, s80, 0x4000\n"
    "s_nop 0\n"
    "buffer_load_dwordx4 v247, s[96:99], s48 offen offset:0 nt lds\n"
    "buffer_load_dwordx4 v247, s[96:99], s49 offen offset:1024 nt lds\n"
    "ds_read_b128 a[60:63], v227 offset:5632\n"
    "v_exp_f32_e64 v173, v173 clamp\n"
    "v_exp_f32_e64 v174, v174 clamp\n"
    "v_exp_f32_e64 v175, v175 clamp\n"
    "v_pk_add_f32 v[160:161], v[160:161], v[192:193]\n"
    "v_pk_add_f32 v[162:163], v[162:163], v[194:195]\n"
    "v_pk_add_f32 v[164:165], v[164:165], v[196:197]\n"
    "v_pk_add_f32 v[166:167], v[166:167], v[198:199]\n"
    "v_pk_add_f32 v[168:169], v[168:169], v[200:201]\n"
    "v_pk_add_f32 v[170:171], v[170:171], v[202:203]\n"
    "v_pk_add_f32 v[172:173], v[172:173], v[204:205]\n"
    "v_pk_add_f32 v[174:175], v[174:175], v[206:207]\n"
    "v_pk_add_f32 v[208:209], v[160:161], v[162:163]\n"
    "v_pk_add_f32 v[210:211], v[164:165], v[166:167]\n"
    "v_pk_add_f32 v[212:213], v[168:169], v[170:171]\n"
    "v_pk_add_f32 v[214:215], v[172:173], v[174:175]\n"
    "v_pk_mul_f32 v[160:161], v[160:161], v[162:163]\n"
    "v_pk_mul_f32 v[164:165], v[164:165], v[166:167]\n"
    "v_pk_mul_f32 v[168:169], v[168:169], v[170:171]\n"
    "v_pk_mul_f32 v[172:173], v[172:173], v[174:175]\n"
    "v_pk_mul_f32 v[208:209], v[208:209], v[164:165]\n"
    "v_pk_mul_f32 v[212:213], v[212:213], v[172:173]\n"
    "v_pk_mul_f32 v[216:217], v[160:161], v[164:165]\n"
    "v_pk_mul_f32 v[218:219], v[168:169], v[172:173]\n"
    "v_pk_fma_f32 v[208:209], v[210:211], v[160:161], v[208:209]\n"
    "v_pk_fma_f32 v[212:213], v[214:215], v[168:169], v[212:213]\n"
    "s_waitcnt lgkmcnt(3)\n"
    "v_mfma_f32_32x32x16_bf16 v[160:175], v[64:67], a[32:35], v[128:143]\n"
    "s_waitcnt vmcnt(8) lgkmcnt(0)\n"
    "s_barrier\n"
    "ds_read_b128 a[32:35], v226 offset:8192\n"
    "v_rcp_f32 v216, v216\n"
    "v_mfma_f32_32x32x16_bf16 v[160:175], v[68:71], a[36:39], v[160:175]\n"
    "ds_read_b128 a[36:39], v227 offset:8192\n"
    "ds_read_b128 v[144:147], v239 offset:3456\n"
    "v_rcp_f32 v217, v217\n"
    "v_rcp_f32 v218, v218\n"
    "v_mfma_f32_32x32x16_bf16 v[160:175], v[72:75], a[40:43], v[160:175]\n"
    "ds_read_b128 a[40:43], v226 offset:8704\n"
    "ds_read_b128 v[148:151], v239 offset:3488\n"
    "v_rcp_f32 v219, v219\n"
    "v_exp_f32_e64 v176, v176 clamp\n"
    "v_mfma_f32_32x32x16_bf16 v[160:175], v[76:79], a[44:47], v[160:175]\n"
    "ds_read_b128 a[44:47], v227 offset:8704\n"
    "ds_read_b128 v[152:155], v239 offset:3520\n"
    "v_exp_f32_e64 v177, v177 clamp\n"
    "v_exp_f32_e64 v178, v178 clamp\n"
    "v_mfma_f32_32x32x16_bf16 v[160:175], v[80:83], a[48:51], v[160:175]\n"
    "ds_read_b128 a[48:51], v226 offset:9216\n"
    "ds_read_b128 v[156:159], v239 offset:3552\n"
    "v_perm_b32 v246, v242, v241, s61\n"
    "v_exp_f32_e64 v179, v179 clamp\n"
    "v_exp_f32_e64 v180, v180 clamp\n"
    "v_mfma_f32_32x32x16_bf16 v[160:175], v[84:87], a[52:55], v[160:175]\n"
    "ds_read_b128 a[52:55], v227 offset:9216\n"
    "v_exp_f32_e64 v181, v181 clamp\n"
    "v_exp_f32_e64 v182, v182 clamp\n"
    "v_mfma_f32_32x32x16_bf16 v[160:175], v[88:91], a[56:59], v[160:175]\n"
    "buffer_load_dwordx4 v246, s[84:87], s50 offen offset:2048 nt lds\n"
    "buffer_load_dwordx4 v246, s[84:87], s51 offen offset:3072 nt lds\n"
    "ds_read_b128 a[56:59], v226 offset:9728\n"
    "v_exp_f32_e64 v183, v183 clamp\n"
    "v_exp_f32_e64 v184, v184 clamp\n"
    "v_mfma_f32_32x32x16_bf16 v[160:175], v[92:95], a[60:63], v[160:175]\n"
    "ds_read_b128 a[60:63], v227 offset:9728\n"
    "v_exp_f32_e64 v185, v185 clamp\n"
    "v_exp_f32_e64 v186, v186 clamp\n"
    "v_exp_f32_e64 v187, v187 clamp\n"
    "v_exp_f32_e64 v188, v188 clamp\n"
    "v_exp_f32_e64 v189, v189 clamp\n"
    "v_exp_f32_e64 v190, v190 clamp\n"
    "v_exp_f32_e64 v191, v191 clamp\n"
    "v_pk_fma_f32 v[220:221], v[208:209], v[216:217], v[222:223]\n"
    "v_pk_add_f32 v[176:177], v[176:177], v[192:193]\n"
    "v_pk_add_f32 v[178:179], v[178:179], v[194:195]\n"
    "v_pk_add_f32 v[180:181], v[180:181], v[196:197]\n"
    "v_pk_add_f32 v[182:183], v[182:183], v[198:199]\n"
    "v_pk_fma_f32 v[220:221], v[212:213], v[218:219], v[220:221]\n"
    "v_pk_add_f32 v[184:185], v[184:185], v[200:201]\n"
    "v_pk_add_f32 v[186:187], v[186:187], v[202:203]\n"
    "v_pk_add_f32 v[188:189], v[188:189], v[204:205]\n"
    "v_pk_add_f32 v[190:191], v[190:191], v[206:207]\n"
    "v_pk_add_f32 v[208:209], v[176:177], v[178:179]\n"
    "v_pk_add_f32 v[210:211], v[180:181], v[182:183]\n"
    "v_pk_add_f32 v[212:213], v[184:185], v[186:187]\n"
    "v_pk_add_f32 v[214:215], v[188:189], v[190:191]\n"
    "v_pk_mul_f32 v[176:177], v[176:177], v[178:179]\n"
    "v_pk_mul_f32 v[180:181], v[180:181], v[182:183]\n"
    "v_pk_mul_f32 v[184:185], v[184:185], v[186:187]\n"
    "v_pk_mul_f32 v[188:189], v[188:189], v[190:191]\n"
    "v_pk_mul_f32 v[208:209], v[208:209], v[180:181]\n"
    "v_pk_mul_f32 v[212:213], v[212:213], v[188:189]\n"
    "v_pk_mul_f32 v[216:217], v[176:177], v[180:181]\n"
    "v_pk_mul_f32 v[218:219], v[184:185], v[188:189]\n"
    "v_pk_fma_f32 v[208:209], v[210:211], v[176:177], v[208:209]\n"
    "v_pk_fma_f32 v[212:213], v[214:215], v[184:185], v[212:213]\n"
    "s_waitcnt lgkmcnt(3)\n"
    "v_mfma_f32_32x32x16_bf16 v[176:191], a[0:3], a[32:35], v[144:159]\n"
    "s_waitcnt vmcnt(8) lgkmcnt(0)\n"
    "s_barrier\n"
    "ds_read_b128 a[32:35], v226 offset:10240\n"
    "v_add_f32 v224, v220, v221\n"
    "v_rcp_f32 v216, v216\n"
    "v_mfma_f32_32x32x16_bf16 v[176:191], a[4:7], a[36:39], v[176:191]\n"
    "ds_read_b128 a[36:39], v227 offset:10240\n"
    "v_cvt_pk_bf16_f32 v224, v224, v224\n"
    "v_rcp_f32 v217, v217\n"
    "v_rcp_f32 v218, v218\n"
    "v_mfma_f32_32x32x16_bf16 v[176:191], a[8:11], a[40:43], v[176:191]\n"
    "ds_read_b128 a[40:43], v226 offset:10752\n"
    "v_rcp_f32 v219, v219\n"
    "v_exp_f32_e64 v160, v160 clamp\n"
    "v_mfma_f32_32x32x16_bf16 v[176:191], a[12:15], a[44:47], v[176:191]\n"
    "ds_read_b128 a[44:47], v227 offset:10752\n"
    "v_exp_f32_e64 v161, v161 clamp\n"
    "v_exp_f32_e64 v162, v162 clamp\n"
    "v_mfma_f32_32x32x16_bf16 v[176:191], a[16:19], a[48:51], v[176:191]\n"
    "ds_read_b128 a[48:51], v226 offset:11264\n"
    "v_perm_b32 v247, v243, v241, s61\n"
    "v_exp_f32_e64 v163, v163 clamp\n"
    "v_exp_f32_e64 v164, v164 clamp\n"
    "v_mfma_f32_32x32x16_bf16 v[176:191], a[20:23], a[52:55], v[176:191]\n"
    "s_add_u32 m0, s80, 0x5000\n"
    "ds_read_b128 a[52:55], v227 offset:11264\n"
    "v_exp_f32_e64 v165, v165 clamp\n"
    "v_exp_f32_e64 v166, v166 clamp\n"
    "v_mfma_f32_32x32x16_bf16 v[176:191], a[24:27], a[56:59], v[176:191]\n"
    "buffer_load_dwordx4 v247, s[88:91], s48 offen offset:0 nt lds\n"
    "buffer_load_dwordx4 v247, s[88:91], s49 offen offset:1024 nt lds\n"
    "ds_read_b128 a[56:59], v226 offset:11776\n"
    "ds_write_b16 v232, v224 offset:0\n"
    "v_exp_f32_e64 v167, v167 clamp\n"
    "v_exp_f32_e64 v168, v168 clamp\n"
    "v_mfma_f32_32x32x16_bf16 v[176:191], a[28:31], a[60:63], v[176:191]\n"
    "ds_read_b128 a[60:63], v227 offset:11776\n"
    "v_exp_f32_e64 v169, v169 clamp\n"
    "v_exp_f32_e64 v170, v170 clamp\n"
    "v_exp_f32_e64 v171, v171 clamp\n"
    "v_exp_f32_e64 v172, v172 clamp\n"
    "v_exp_f32_e64 v173, v173 clamp\n"
    "v_exp_f32_e64 v174, v174 clamp\n"
    "v_exp_f32_e64 v175, v175 clamp\n"
    "v_pk_fma_f32 v[220:221], v[208:209], v[216:217], v[222:223]\n"
    "v_pk_add_f32 v[160:161], v[160:161], v[192:193]\n"
    "v_pk_add_f32 v[162:163], v[162:163], v[194:195]\n"
    "v_pk_add_f32 v[164:165], v[164:165], v[196:197]\n"
    "v_pk_add_f32 v[166:167], v[166:167], v[198:199]\n"
    "v_pk_fma_f32 v[220:221], v[212:213], v[218:219], v[220:221]\n"
    "v_pk_add_f32 v[168:169], v[168:169], v[200:201]\n"
    "v_pk_add_f32 v[170:171], v[170:171], v[202:203]\n"
    "v_pk_add_f32 v[172:173], v[172:173], v[204:205]\n"
    "v_pk_add_f32 v[174:175], v[174:175], v[206:207]\n"
    "v_pk_add_f32 v[208:209], v[160:161], v[162:163]\n"
    "v_pk_add_f32 v[210:211], v[164:165], v[166:167]\n"
    "v_pk_add_f32 v[212:213], v[168:169], v[170:171]\n"
    "v_pk_add_f32 v[214:215], v[172:173], v[174:175]\n"
    "v_pk_mul_f32 v[160:161], v[160:161], v[162:163]\n"
    "v_pk_mul_f32 v[164:165], v[164:165], v[166:167]\n"
    "v_pk_mul_f32 v[168:169], v[168:169], v[170:171]\n"
    "v_pk_mul_f32 v[172:173], v[172:173], v[174:175]\n"
    "v_pk_mul_f32 v[208:209], v[208:209], v[164:165]\n"
    "v_pk_mul_f32 v[212:213], v[212:213], v[172:173]\n"
    "v_pk_mul_f32 v[216:217], v[160:161], v[164:165]\n"
    "v_pk_mul_f32 v[218:219], v[168:169], v[172:173]\n"
    "v_pk_fma_f32 v[208:209], v[210:211], v[160:161], v[208:209]\n"
    "v_pk_fma_f32 v[212:213], v[214:215], v[168:169], v[212:213]\n"
    "s_waitcnt lgkmcnt(8)\n"
    "v_mfma_f32_32x32x16_bf16 v[160:175], v[0:3], a[32:35], v[96:111]\n"
    "s_waitcnt vmcnt(8) lgkmcnt(0)\n"
    "s_barrier\n"
    "ds_read_b128 a[32:35], v226 offset:12288\n"
    "v_add_f32 v224, v220, v221\n"
    "v_rcp_f32 v216, v216\n"
    "v_mfma_f32_32x32x16_bf16 v[160:175], v[4:7], a[36:39], v[160:175]\n"
    "ds_read_b128 a[36:39], v227 offset:12288\n"
    "ds_read_b128 a[64:67], v234 offset:0\n"
    "v_cvt_pk_bf16_f32 v224, v224, v224\n"
    "v_rcp_f32 v217, v217\n"
    "v_rcp_f32 v218, v218\n"
    "v_mfma_f32_32x32x16_bf16 v[160:175], v[8:11], a[40:43], v[160:175]\n"
    "ds_read_b128 a[40:43], v226 offset:12800\n"
    "ds_read_b64_tr_b16 a[68:69], v228 offset:0\n"
    "v_rcp_f32 v219, v219\n"
    "v_exp_f32_e64 v176, v176 clamp\n"
    "v_mfma_f32_32x32x16_bf16 v[160:175], v[12:15], a[44:47], v[160:175]\n"
    "ds_read_b128 a[44:47], v227 offset:12800\n"
    "ds_read_b64_tr_b16 a[70:71], v229 offset:0\n"
    "v_exp_f32_e64 v177, v177 clamp\n"
    "v_exp_f32_e64 v178, v178 clamp\n"
    "v_mfma_f32_32x32x16_bf16 v[160:175], v[16:19], a[48:51], v[160:175]\n"
    "ds_read_b128 a[48:51], v226 offset:13312\n"
    "ds_read_b64_tr_b16 a[72:73], v230 offset:0\n"
    "v_perm_b32 v246, v244, v241, s61\n"
    "v_exp_f32_e64 v179, v179 clamp\n"
    "v_exp_f32_e64 v180, v180 clamp\n"
    "v_mfma_f32_32x32x16_bf16 v[160:175], v[20:23], a[52:55], v[160:175]\n"
    "ds_read_b128 a[52:55], v227 offset:13312\n"
    "ds_read_b64_tr_b16 a[74:75], v231 offset:0\n"
    "v_exp_f32_e64 v181, v181 clamp\n"
    "v_exp_f32_e64 v182, v182 clamp\n"
    "v_mfma_f32_32x32x16_bf16 v[160:175], v[24:27], a[56:59], v[160:175]\n"
    "ds_read_b128 a[56:59], v226 offset:13824\n"
    "ds_write_b16 v232, v224 offset:40960\n"
    "v_exp_f32_e64 v183, v183 clamp\n"
    "v_exp_f32_e64 v184, v184 clamp\n"
    "v_mfma_f32_32x32x16_bf16 v[160:175], v[28:31], a[60:63], v[160:175]\n"
    "s_waitcnt lgkmcnt(2)\n"
    "ds_read_b128 a[60:63], v227 offset:13824\n"
    "v_exp_f32_e64 v185, v185 clamp\n"
    "v_exp_f32_e64 v186, v186 clamp\n"
    "v_exp_f32_e64 v187, v187 clamp\n"
    "v_mfma_f32_16x16x32_bf16 a[76:79], a[64:67], a[68:71], 0\n"
    "buffer_load_dwordx4 v246, s[92:95], s50 offen offset:2048 nt lds\n"
    "buffer_load_dwordx4 v246, s[92:95], s51 offen offset:3072 nt lds\n"
    "v_exp_f32_e64 v188, v188 clamp\n"
    "v_exp_f32_e64 v189, v189 clamp\n"
    "v_mfma_f32_16x16x32_bf16 a[80:83], a[64:67], a[72:75], 0\n"
    "v_exp_f32_e64 v190, v190 clamp\n"
    "v_exp_f32_e64 v191, v191 clamp\n"
    "v_pk_fma_f32 v[220:221], v[208:209], v[216:217], v[222:223]\n"
    "v_pk_add_f32 v[176:177], v[176:177], v[192:193]\n"
    "v_pk_add_f32 v[178:179], v[178:179], v[194:195]\n"
    "v_pk_add_f32 v[180:181], v[180:181], v[196:197]\n"
    "v_pk_add_f32 v[182:183], v[182:183], v[198:199]\n"
    "v_pk_fma_f32 v[220:221], v[212:213], v[218:219], v[220:221]\n"
    "v_pk_add_f32 v[184:185], v[184:185], v[200:201]\n"
    "v_pk_add_f32 v[186:187], v[186:187], v[202:203]\n"
    "v_pk_add_f32 v[188:189], v[188:189], v[204:205]\n"
    "v_pk_add_f32 v[190:191], v[190:191], v[206:207]\n"
    "v_pk_add_f32 v[208:209], v[176:177], v[178:179]\n"
    "v_pk_add_f32 v[210:211], v[180:181], v[182:183]\n"
    "v_pk_add_f32 v[212:213], v[184:185], v[186:187]\n"
    "v_pk_add_f32 v[214:215], v[188:189], v[190:191]\n"
    "v_pk_mul_f32 v[176:177], v[176:177], v[178:179]\n"
    "v_pk_mul_f32 v[180:181], v[180:181], v[182:183]\n"
    "v_pk_mul_f32 v[184:185], v[184:185], v[186:187]\n"
    "v_pk_mul_f32 v[188:189], v[188:189], v[190:191]\n"
    "v_pk_mul_f32 v[208:209], v[208:209], v[180:181]\n"
    "v_pk_mul_f32 v[212:213], v[212:213], v[188:189]\n"
    "v_pk_mul_f32 v[216:217], v[176:177], v[180:181]\n"
    "v_pk_mul_f32 v[218:219], v[184:185], v[188:189]\n"
    "v_pk_fma_f32 v[208:209], v[210:211], v[176:177], v[208:209]\n"
    "v_pk_fma_f32 v[212:213], v[214:215], v[184:185], v[212:213]\n"
    "v_mfma_f32_32x32x16_bf16 v[176:191], v[32:35], a[32:35], v[112:127]\n"
    "s_waitcnt vmcnt(8) lgkmcnt(0)\n"
    "s_barrier\n"
    "ds_read_b128 a[32:35], v226 offset:14336\n"
    "v_add_f32 v224, v220, v221\n"
    "v_rcp_f32 v216, v216\n"
    "v_mfma_f32_32x32x16_bf16 v[176:191], v[36:39], a[36:39], v[176:191]\n"
    "ds_read_b128 a[36:39], v227 offset:14336\n"
    "ds_read_b128 a[64:67], v235 offset:0\n"
    "v_cvt_pk_bf16_f32 v224, v224, v224\n"
    "v_rcp_f32 v217, v217\n"
    "v_rcp_f32 v218, v218\n"
    "v_mfma_f32_32x32x16_bf16 v[176:191], v[40:43], a[40:43], v[176:191]\n"
    "ds_read_b128 a[40:43], v226 offset:14848\n"
    "ds_read_b64_tr_b16 a[68:69], v228 offset:2048\n"
    "v_rcp_f32 v219, v219\n"
    "v_exp_f32_e64 v160, v160 clamp\n"
    "v_mfma_f32_32x32x16_bf16 v[176:191], v[44:47], a[44:47], v[176:191]\n"
    "ds_read_b128 a[44:47], v227 offset:14848\n"
    "ds_read_b64_tr_b16 a[70:71], v229 offset:2048\n"
    "v_exp_f32_e64 v161, v161 clamp\n"
    "v_exp_f32_e64 v162, v162 clamp\n"
    "v_mfma_f32_32x32x16_bf16 v[176:191], v[48:51], a[48:51], v[176:191]\n"
    "ds_read_b128 a[48:51], v226 offset:15360\n"
    "ds_read_b64_tr_b16 a[72:73], v230 offset:2048\n"
    "v_perm_b32 v247, v245, v241, s61\n"
    "v_exp_f32_e64 v163, v163 clamp\n"
    "v_exp_f32_e64 v164, v164 clamp\n"
    "v_mfma_f32_32x32x16_bf16 v[176:191], v[52:55], a[52:55], v[176:191]\n"
    "s_add_u32 m0, s80, 0x6000\n"
    "ds_read_b128 a[52:55], v227 offset:15360\n"
    "ds_read_b64_tr_b16 a[74:75], v231 offset:2048\n"
    "v_exp_f32_e64 v165, v165 clamp\n"
    "v_exp_f32_e64 v166, v166 clamp\n"
    "v_mfma_f32_32x32x16_bf16 v[176:191], v[56:59], a[56:59], v[176:191]\n"
    "ds_read_b128 a[56:59], v226 offset:15872\n"
    "ds_write_b16 v233, v224 offset:0\n"
    "v_exp_f32_e64 v167, v167 clamp\n"
    "v_exp_f32_e64 v168, v168 clamp\n"
    "v_mfma_f32_32x32x16_bf16 v[176:191], v[60:63], a[60:63], v[176:191]\n"
    "s_waitcnt lgkmcnt(2)\n"
    "ds_read_b128 a[60:63], v227 offset:15872\n"
    "v_exp_f32_e64 v169, v169 clamp\n"
    "v_exp_f32_e64 v170, v170 clamp\n"
    "v_exp_f32_e64 v171, v171 clamp\n"
    "v_mfma_f32_16x16x32_bf16 a[76:79], a[64:67], a[68:71], a[76:79]\n"
    "buffer_load_dwordx4 v247, s[96:99], s48 offen offset:0 nt lds\n"
    "buffer_load_dwordx4 v247, s[96:99], s49 offen offset:1024 nt lds\n"
    "v_exp_f32_e64 v172, v172 clamp\n"
    "v_exp_f32_e64 v173, v173 clamp\n"
    "v_mfma_f32_16x16x32_bf16 a[80:83], a[64:67], a[72:75], a[80:83]\n"
    "v_exp_f32_e64 v174, v174 clamp\n"
    "v_exp_f32_e64 v175, v175 clamp\n"
    "v_pk_fma_f32 v[220:221], v[208:209], v[216:217], v[222:223]\n"
    "v_pk_add_f32 v[160:161], v[160:161], v[192:193]\n"
    "v_pk_add_f32 v[162:163], v[162:163], v[194:195]\n"
    "v_pk_add_f32 v[164:165], v[164:165], v[196:197]\n"
    "v_pk_add_f32 v[166:167], v[166:167], v[198:199]\n"
    "v_pk_fma_f32 v[220:221], v[212:213], v[218:219], v[220:221]\n"
    "v_pk_add_f32 v[168:169], v[168:169], v[200:201]\n"
    "v_pk_add_f32 v[170:171], v[170:171], v[202:203]\n"
    "v_pk_add_f32 v[172:173], v[172:173], v[204:205]\n"
    "v_pk_add_f32 v[174:175], v[174:175], v[206:207]\n"
    "v_pk_add_f32 v[208:209], v[160:161], v[162:163]\n"
    "v_pk_add_f32 v[210:211], v[164:165], v[166:167]\n"
    "v_pk_add_f32 v[212:213], v[168:169], v[170:171]\n"
    "v_pk_add_f32 v[214:215], v[172:173], v[174:175]\n"
    "v_pk_mul_f32 v[160:161], v[160:161], v[162:163]\n"
    "v_pk_mul_f32 v[164:165], v[164:165], v[166:167]\n"
    "v_pk_mul_f32 v[168:169], v[168:169], v[170:171]\n"
    "v_pk_mul_f32 v[172:173], v[172:173], v[174:175]\n"
    "v_pk_mul_f32 v[208:209], v[208:209], v[164:165]\n"
    "v_pk_mul_f32 v[212:213], v[212:213], v[172:173]\n"
    "v_pk_mul_f32 v[216:217], v[160:161], v[164:165]\n"
    "v_pk_mul_f32 v[218:219], v[168:169], v[172:173]\n"
    "v_pk_fma_f32 v[208:209], v[210:211], v[160:161], v[208:209]\n"
    "v_pk_fma_f32 v[212:213], v[214:215], v[168:169], v[212:213]\n"
    "v_mfma_f32_32x32x16_bf16 v[160:175], v[64:67], a[32:35], v[128:143]\n"
    "s_waitcnt vmcnt(8) lgkmcnt(0)\n"
    "s_barrier\n"
    "ds_read_b128 a[32:35], v226 offset:16384\n"
    "v_add_f32 v224, v220, v221\n"
    "v_rcp_f32 v216, v216\n"
    "v_mfma_f32_32x32x16_bf16 v[160:175], v[68:71], a[36:39], v[160:175]\n"
    "ds_read_b128 a[36:39], v227 offset:16384\n"
    "ds_read_b128 a[64:67], v236 offset:0\n"
    "v_cvt_pk_bf16_f32 v224, v224, v224\n"
    "v_rcp_f32 v217, v217\n"
    "v_rcp_f32 v218, v218\n"
    "v_mfma_f32_32x32x16_bf16 v[160:175], v[72:75], a[40:43], v[160:175]\n"
    "ds_read_b128 a[40:43], v226 offset:16896\n"
    "ds_read_b64_tr_b16 a[68:69], v228 offset:4096\n"
    "v_rcp_f32 v219, v219\n"
    "v_exp_f32_e64 v176, v176 clamp\n"
    "v_mfma_f32_32x32x16_bf16 v[160:175], v[76:79], a[44:47], v[160:175]\n"
    "ds_read_b128 a[44:47], v227 offset:16896\n"
    "ds_read_b64_tr_b16 a[70:71], v229 offset:4096\n"
    "v_exp_f32_e64 v177, v177 clamp\n"
    "v_exp_f32_e64 v178, v178 clamp\n"
    "v_mfma_f32_32x32x16_bf16 v[160:175], v[80:83], a[48:51], v[160:175]\n"
    "ds_read_b128 a[48:51], v226 offset:17408\n"
    "ds_read_b64_tr_b16 a[72:73], v230 offset:4096\n"
    "v_perm_b32 v246, v242, v241, s62\n"
    "v_exp_f32_e64 v179, v179 clamp\n"
    "v_exp_f32_e64 v180, v180 clamp\n"
    "v_mfma_f32_32x32x16_bf16 v[160:175], v[84:87], a[52:55], v[160:175]\n"
    "ds_read_b128 a[52:55], v227 offset:17408\n"
    "ds_read_b64_tr_b16 a[74:75], v231 offset:4096\n"
    "v_exp_f32_e64 v181, v181 clamp\n"
    "v_exp_f32_e64 v182, v182 clamp\n"
    "v_mfma_f32_32x32x16_bf16 v[160:175], v[88:91], a[56:59], v[160:175]\n"
    "ds_read_b128 a[56:59], v226 offset:17920\n"
    "ds_write_b16 v233, v224 offset:40960\n"
    "v_exp_f32_e64 v183, v183 clamp\n"
    "v_exp_f32_e64 v184, v184 clamp\n"
    "v_mfma_f32_32x32x16_bf16 v[160:175], v[92:95], a[60:63], v[160:175]\n"
    "s_waitcnt lgkmcnt(2)\n"
    "ds_read_b128 a[60:63], v227 offset:17920\n"
    "v_exp_f32_e64 v185, v185 clamp\n"
    "v_exp_f32_e64 v186, v186 clamp\n"
    "v_exp_f32_e64 v187, v187 clamp\n"
    "v_mfma_f32_16x16x32_bf16 a[84:87], a[64:67], a[68:71], 0\n"
    "buffer_load_dwordx4 v246, s[84:87], s50 offen offset:2048 nt lds\n"
    "buffer_load_dwordx4 v246, s[84:87], s51 offen offset:3072 nt lds\n"
    "v_exp_f32_e64 v188, v188 clamp\n"
    "v_exp_f32_e64 v189, v189 clamp\n"
    "v_mfma_f32_16x16x32_bf16 a[88:91], a[64:67], a[72:75], 0\n"
    "v_exp_f32_e64 v190, v190 clamp\n"
    "v_exp_f32_e64 v191, v191 clamp\n"
    "v_pk_fma_f32 v[220:221], v[208:209], v[216:217], v[222:223]\n"
    "v_pk_add_f32 v[176:177], v[176:177], v[192:193]\n"
    "v_pk_add_f32 v[178:179], v[178:179], v[194:195]\n"
    "v_pk_add_f32 v[180:181], v[180:181], v[196:197]\n"
    "v_pk_add_f32 v[182:183], v[182:183], v[198:199]\n"
    "v_pk_fma_f32 v[220:221], v[212:213], v[218:219], v[220:221]\n"
    "v_pk_add_f32 v[184:185], v[184:185], v[200:201]\n"
    "v_pk_add_f32 v[186:187], v[186:187], v[202:203]\n"
    "v_pk_add_f32 v[188:189], v[188:189], v[204:205]\n"
    "v_pk_add_f32 v[190:191], v[190:191], v[206:207]\n"
    "v_pk_add_f32 v[208:209], v[176:177], v[178:179]\n"
    "v_pk_add_f32 v[210:211], v[180:181], v[182:183]\n"
    "v_pk_add_f32 v[212:213], v[184:185], v[186:187]\n"
    "v_pk_add_f32 v[214:215], v[188:189], v[190:191]\n"
    "v_pk_mul_f32 v[176:177], v[176:177], v[178:179]\n"
    "v_pk_mul_f32 v[180:181], v[180:181], v[182:183]\n"
    "v_pk_mul_f32 v[184:185], v[184:185], v[186:187]\n"
    "v_pk_mul_f32 v[188:189], v[188:189], v[190:191]\n"
    "v_pk_mul_f32 v[208:209], v[208:209], v[180:181]\n"
    "v_pk_mul_f32 v[212:213], v[212:213], v[188:189]\n"
    "v_pk_mul_f32 v[216:217], v[176:177], v[180:181]\n"
    "v_pk_mul_f32 v[218:219], v[184:185], v[188:189]\n"
    "v_pk_fma_f32 v[208:209], v[210:211], v[176:177], v[208:209]\n"
    "v_pk_fma_f32 v[212:213], v[214:215], v[184:185], v[212:213]\n"
    "v_mfma_f32_32x32x16_bf16 v[176:191], a[0:3], a[32:35], v[144:159]\n"
    "s_waitcnt vmcnt(8) lgkmcnt(0)\n"
    "s_barrier\n"
    "ds_read_b128 a[32:35], v226 offset:18432\n"
    "v_add_f32 v224, v220, v221\n"
    "v_rcp_f32 v216, v216\n"
    "v_mfma_f32_32x32x16_bf16 v[176:191], a[4:7], a[36:39], v[176:191]\n"
    "ds_read_b128 a[36:39], v227 offset:18432\n"
    "ds_read_b128 a[64:67], v237 offset:0\n"
    "v_cvt_pk_bf16_f32 v224, v224, v224\n"
    "v_rcp_f32 v217, v217\n"
    "v_rcp_f32 v218, v218\n"
    "v_mfma_f32_32x32x16_bf16 v[176:191], a[8:11], a[40:43], v[176:191]\n"
    "ds_read_b128 a[40:43], v226 offset:18944\n"
    "ds_read_b64_tr_b16 a[68:69], v228 offset:8192\n"
    "v_rcp_f32 v219, v219\n"
    "v_exp_f32_e64 v160, v160 clamp\n"
    "v_mfma_f32_32x32x16_bf16 v[176:191], a[12:15], a[44:47], v[176:191]\n"
    "ds_read_b128 a[44:47], v227 offset:18944\n"
    "ds_read_b64_tr_b16 a[70:71], v229 offset:8192\n"
    "v_exp_f32_e64 v161, v161 clamp\n"
    "v_exp_f32_e64 v162, v162 clamp\n"
    "v_mfma_f32_32x32x16_bf16 v[176:191], a[16:19], a[48:51], v[176:191]\n"
    "ds_read_b128 a[48:51], v226 offset:19456\n"
    "ds_read_b64_tr_b16 a[72:73], v230 offset:8192\n"
    "v_perm_b32 v247, v243, v241, s62\n"
    "v_exp_f32_e64 v163, v163 clamp\n"
    "v_exp_f32_e64 v164, v164 clamp\n"
    "v_mfma_f32_32x32x16_bf16 v[176:191], a[20:23], a[52:55], v[176:191]\n"
    "s_add_u32 m0, s80, 0x7000\n"
    "ds_read_b128 a[52:55], v227 offset:19456\n"
    "ds_read_b64_tr_b16 a[74:75], v231 offset:8192\n"
    "v_exp_f32_e64 v165, v165 clamp\n"
    "v_exp_f32_e64 v166, v166 clamp\n"
    "v_mfma_f32_32x32x16_bf16 v[176:191], a[24:27], a[56:59], v[176:191]\n"
    "ds_read_b128 a[56:59], v226 offset:19968\n"
    "ds_write_b16 v232, v224 offset:64\n"
    "v_exp_f32_e64 v167, v167 clamp\n"
    "v_exp_f32_e64 v168, v168 clamp\n"
    "v_mfma_f32_32x32x16_bf16 v[176:191], a[28:31], a[60:63], v[176:191]\n"
    "s_waitcnt lgkmcnt(2)\n"
    "ds_read_b128 a[60:63], v227 offset:19968\n"
    "v_exp_f32_e64 v169, v169 clamp\n"
    "v_exp_f32_e64 v170, v170 clamp\n"
    "v_exp_f32_e64 v171, v171 clamp\n"
    "v_mfma_f32_16x16x32_bf16 a[84:87], a[64:67], a[68:71], a[84:87]\n"
    "buffer_load_dwordx4 v247, s[88:91], s48 offen offset:0 nt lds\n"
    "buffer_load_dwordx4 v247, s[88:91], s49 offen offset:1024 nt lds\n"
    "v_exp_f32_e64 v172, v172 clamp\n"
    "v_exp_f32_e64 v173, v173 clamp\n"
    "v_mfma_f32_16x16x32_bf16 a[88:91], a[64:67], a[72:75], a[88:91]\n"
    "v_exp_f32_e64 v174, v174 clamp\n"
    "v_exp_f32_e64 v175, v175 clamp\n"
    "v_pk_fma_f32 v[220:221], v[208:209], v[216:217], v[222:223]\n"
    "v_pk_add_f32 v[160:161], v[160:161], v[192:193]\n"
    "v_pk_add_f32 v[162:163], v[162:163], v[194:195]\n"
    "v_pk_add_f32 v[164:165], v[164:165], v[196:197]\n"
    "v_pk_add_f32 v[166:167], v[166:167], v[198:199]\n"
    "v_pk_fma_f32 v[220:221], v[212:213], v[218:219], v[220:221]\n"
    "v_pk_add_f32 v[168:169], v[168:169], v[200:201]\n"
    "v_pk_add_f32 v[170:171], v[170:171], v[202:203]\n"
    "v_pk_add_f32 v[172:173], v[172:173], v[204:205]\n"
    "v_pk_add_f32 v[174:175], v[174:175], v[206:207]\n"
    "v_pk_add_f32 v[208:209], v[160:161], v[162:163]\n"
    "v_pk_add_f32 v[210:211], v[164:165], v[166:167]\n"
    "v_pk_add_f32 v[212:213], v[168:169], v[170:171]\n"
    "v_pk_add_f32 v[214:215], v[172:173], v[174:175]\n"
    "v_pk_mul_f32 v[160:161], v[160:161], v[162:163]\n"
    "v_pk_mul_f32 v[164:165], v[164:165], v[166:167]\n"
    "v_pk_mul_f32 v[168:169], v[168:169], v[170:171]\n"
    "v_pk_mul_f32 v[172:173], v[172:173], v[174:175]\n"
    "v_pk_mul_f32 v[208:209], v[208:209], v[164:165]\n"
    "v_pk_mul_f32 v[212:213], v[212:213], v[172:173]\n"
    "v_pk_mul_f32 v[216:217], v[160:161], v[164:165]\n"
    "v_pk_mul_f32 v[218:219], v[168:169], v[172:173]\n"
    "v_pk_fma_f32 v[208:209], v[210:211], v[160:161], v[208:209]\n"
    "v_pk_fma_f32 v[212:213], v[214:215], v[168:169], v[212:213]\n"
    "s_cmp_lg_u32 s35, 0\n"
    "s_cbranch_scc1 L_ext_%=\n"
    "v_mfma_f32_32x32x16_bf16 v[160:175], v[0:3], a[32:35], v[96:111]\n"
    "s_waitcnt vmcnt(8) lgkmcnt(0)\n"
    "s_barrier\n"
    "ds_read_b128 a[32:35], v226 offset:20480\n"
    "v_add_f32 v224, v220, v221\n"
    "v_rcp_f32 v216, v216\n"
    "v_mfma_f32_32x32x16_bf16 v[160:175], v[4:7], a[36:39], v[160:175]\n"
    "ds_read_b128 a[36:39], v227 offset:20480\n"
    "ds_read_b128 a[64:67], v234 offset:64\n"
    "v_cvt_pk_bf16_f32 v224, v224, v224\n"
    "v_rcp_f32 v217, v217\n"
    "v_rcp_f32 v218, v218\n"
    "v_mfma_f32_32x32x16_bf16 v[160:175], v[8:11], a[40:43], v[160:175]\n"
    "ds_read_b128 a[40:43], v226 offset:20992\n"
    "ds_read_b64_tr_b16 a[68:69], v228 offset:10240\n"
    "v_rcp_f32 v219, v219\n"
    "v_exp_f32_e64 v176, v176 clamp\n"
    "v_mfma_f32_32x32x16_bf16 v[160:175], v[12:15], a[44:47], v[160:175]\n"
    "ds_read_b128 a[44:47], v227 offset:20992\n"
    "ds_read_b64_tr_b16 a[70:71], v229 offset:10240\n"
    "v_exp_f32_e64 v177, v177 clamp\n"
    "v_exp_f32_e64 v178, v178 clamp\n"
    "v_mfma_f32_32x32x16_bf16 v[160:175], v[16:19], a[48:51], v[160:175]\n"
    "ds_read_b128 a[48:51], v226 offset:21504\n"
    "ds_read_b64_tr_b16 a[72:73], v230 offset:10240\n"
    "v_perm_b32 v246, v244, v241, s62\n"
    "v_exp_f32_e64 v179, v179 clamp\n"
    "v_exp_f32_e64 v180, v180 clamp\n"
    "v_mfma_f32_32x32x16_bf16 v[160:175], v[20:23], a[52:55], v[160:175]\n"
    "ds_read_b128 a[52:55], v227 offset:21504\n"
    "ds_read_b64_tr_b16 a[74:75], v231 offset:10240\n"
    "v_exp_f32_e64 v181, v181 clamp\n"
    "v_exp_f32_e64 v182, v182 clamp\n"
    "v_mfma_f32_32x32x16_bf16 v[160:175], v[24:27], a[56:59], v[160:175]\n"
    "ds_read_b128 a[56:59], v226 offset:22016\n"
    "ds_write_b16 v232, v224 offset:41024\n"
    "v_exp_f32_e64 v183, v183 clamp\n"
    "v_exp_f32_e64 v184, v184 clamp\n"
    "v_mfma_f32_32x32x16_bf16 v[160:175], v[28:31], a[60:63], v[160:175]\n"
    "s_waitcnt lgkmcnt(2)\n"
    "ds_read_b128 a[60:63], v227 offset:22016\n"
    "v_exp_f32_e64 v185, v185 clamp\n"
    "v_exp_f32_e64 v186, v186 clamp\n"
    "v_exp_f32_e64 v187, v187 clamp\n"
    "v_mfma_f32_16x16x32_bf16 a[76:79], a[64:67], a[68:71], a[76:79]\n"
    "buffer_load_dwordx4 v246, s[92:95], s50 offen offset:2048 nt lds\n"
    "buffer_load_dwordx4 v246, s[92:95], s51 offen offset:3072 nt lds\n"
    "v_exp_f32_e64 v188, v188 clamp\n"
    "v_exp_f32_e64 v189, v189 clamp\n"
    "v_mfma_f32_16x16x32_bf16 a[80:83], a[64:67], a[72:75], a[80:83]\n"
    "v_exp_f32_e64 v190, v190 clamp\n"
    "v_exp_f32_e64 v191, v191 clamp\n"
    "v_pk_fma_f32 v[220:221], v[208:209], v[216:217], v[222:223]\n"
    "v_pk_add_f32 v[176:177], v[176:177], v[192:193]\n"
    "v_pk_add_f32 v[178:179], v[178:179], v[194:195]\n"
    "v_pk_add_f32 v[180:181], v[180:181], v[196:197]\n"
    "v_pk_add_f32 v[182:183], v[182:183], v[198:199]\n"
    "v_pk_fma_f32 v[220:221], v[212:213], v[218:219], v[220:221]\n"
    "v_pk_add_f32 v[184:185], v[184:185], v[200:201]\n"
    "v_pk_add_f32 v[186:187], v[186:187], v[202:203]\n"
    "v_pk_add_f32 v[188:189], v[188:189], v[204:205]\n"
    "v_pk_add_f32 v[190:191], v[190:191], v[206:207]\n"
    "v_pk_add_f32 v[208:209], v[176:177], v[178:179]\n"
    "v_pk_add_f32 v[210:211], v[180:181], v[182:183]\n"
    "v_pk_add_f32 v[212:213], v[184:185], v[186:187]\n"
    "v_pk_add_f32 v[214:215], v[188:189], v[190:191]\n"
    "v_pk_mul_f32 v[176:177], v[176:177], v[178:179]\n"
    "v_pk_mul_f32 v[180:181], v[180:181], v[182:183]\n"
    "v_pk_mul_f32 v[184:185], v[184:185], v[186:187]\n"
    "v_pk_mul_f32 v[188:189], v[188:189], v[190:191]\n"
    "v_pk_mul_f32 v[208:209], v[208:209], v[180:181]\n"
    "v_pk_mul_f32 v[212:213], v[212:213], v[188:189]\n"
    "v_pk_mul_f32 v[216:217], v[176:177], v[180:181]\n"
    "v_pk_mul_f32 v[218:219], v[184:185], v[188:189]\n"
    "v_pk_fma_f32 v[208:209], v[210:211], v[176:177], v[208:209]\n"
    "v_pk_fma_f32 v[212:213], v[214:215], v[184:185], v[212:213]\n"
    "v_mfma_f32_32x32x16_bf16 v[176:191], v[32:35], a[32:35], v[112:127]\n"
    "s_waitcnt vmcnt(8) lgkmcnt(0)\n"
    "s_barrier\n"
    "ds_read_b128 a[32:35], v226 offset:22528\n"
    "v_add_f32 v224, v220, v221\n"
    "v_rcp_f32 v216, v216\n"
    "v_mfma_f32_32x32x16_bf16 v[176:191], v[36:39], a[36:39], v[176:191]\n"
    "ds_read_b128 a[36:39], v227 offset:22528\n"
    "ds_read_b128 a[64:67], v235 offset:64\n"
    "v_cvt_pk_bf16_f32 v224, v224, v224\n"
    "v_rcp_f32 v217, v217\n"
    "v_rcp_f32 v218, v218\n"
    "v_mfma_f32_32x32x16_bf16 v[176:191], v[40:43], a[40:43], v[176:191]\n"
    "ds_read_b128 a[40:43], v226 offset:23040\n"
    "ds_read_b64_tr_b16 a[68:69], v228 offset:12288\n"
    "v_rcp_f32 v219, v219\n"
    "v_exp_f32_e64 v160, v160 clamp\n"
    "v_mfma_f32_32x32x16_bf16 v[176:191], v[44:47], a[44:47], v[176:191]\n"
    "ds_read_b128 a[44:47], v227 offset:23040\n"
    "ds_read_b64_tr_b16 a[70:71], v229 offset:12288\n"
    "v_exp_f32_e64 v161, v161 clamp\n"
    "v_exp_f32_e64 v162, v162 clamp\n"
    "v_mfma_f32_32x32x16_bf16 v[176:191], v[48:51], a[48:51], v[176:191]\n"
    "ds_read_b128 a[48:51], v226 offset:23552\n"
    "ds_read_b64_tr_b16 a[72:73], v230 offset:12288\n"
    "v_perm_b32 v247, v245, v241, s62\n"
    "v_exp_f32_e64 v163, v163 clamp\n"
    "v_exp_f32_e64 v164, v164 clamp\n"
    "v_mfma_f32_32x32x16_bf16 v[176:191], v[52:55], a[52:55], v[176:191]\n"
    "s_add_u32 m0, s80, 0x1000\n"
    "ds_read_b128 a[52:55], v227 offset:23552\n"
    "ds_read_b64_tr_b16 a[74:75], v231 offset:12288\n"
    "v_exp_f32_e64 v165, v165 clamp\n"
    "v_exp_f32_e64 v166, v166 clamp\n"
    "v_mfma_f32_32x32x16_bf16 v[176:191], v[56:59], a[56:59], v[176:191]\n"
    "ds_read_b128 a[56:59], v226 offset:24064\n"
    "ds_write_b16 v233, v224 offset:64\n"
    "v_exp_f32_e64 v167, v167 clamp\n"
    "v_exp_f32_e64 v168, v168 clamp\n"
    "v_mfma_f32_32x32x16_bf16 v[176:191], v[60:63], a[60:63], v[176:191]\n"
    "s_waitcnt lgkmcnt(2)\n"
    "ds_read_b128 a[60:63], v227 offset:24064\n"
    "v_exp_f32_e64 v169, v169 clamp\n"
    "v_exp_f32_e64 v170, v170 clamp\n"
    "v_exp_f32_e64 v171, v171 clamp\n"
    "v_mfma_f32_16x16x32_bf16 a[76:79], a[64:67], a[68:71], a[76:79]\n"
    "buffer_load_dwordx4 v247, s[96:99], s50 offen offset:2048 nt lds\n"
    "buffer_load_dwordx4 v247, s[96:99], s51 offen offset:3072 nt lds\n"
    "v_exp_f32_e64 v172, v172 clamp\n"
    "v_exp_f32_e64 v173, v173 clamp\n"
    "v_mfma_f32_16x16x32_bf16 a[80:83], a[64:67], a[72:75], a[80:83]\n"
    "v_exp_f32_e64 v174, v174 clamp\n"
    "v_exp_f32_e64 v175, v175 clamp\n"
    "v_pk_fma_f32 v[220:221], v[208:209], v[216:217], v[222:223]\n"
    "v_pk_add_f32 v[160:161], v[160:161], v[192:193]\n"
    "v_pk_add_f32 v[162:163], v[162:163], v[194:195]\n"
    "v_pk_add_f32 v[164:165], v[164:165], v[196:197]\n"
    "v_pk_add_f32 v[166:167], v[166:167], v[198:199]\n"
    "v_pk_fma_f32 v[220:221], v[212:213], v[218:219], v[220:221]\n"
    "v_pk_add_f32 v[168:169], v[168:169], v[200:201]\n"
    "v_pk_add_f32 v[170:171], v[170:171], v[202:203]\n"
    "v_pk_add_f32 v[172:173], v[172:173], v[204:205]\n"
    "v_pk_add_f32 v[174:175], v[174:175], v[206:207]\n"
    "v_pk_add_f32 v[208:209], v[160:161], v[162:163]\n"
    "v_pk_add_f32 v[210:211], v[164:165], v[166:167]\n"
    "v_pk_add_f32 v[212:213], v[168:169], v[170:171]\n"
    "v_pk_add_f32 v[214:215], v[172:173], v[174:175]\n"
    "v_pk_mul_f32 v[160:161], v[160:161], v[162:163]\n"
    "v_pk_mul_f32 v[164:165], v[164:165], v[166:167]\n"
    "v_pk_mul_f32 v[168:169], v[168:169], v[170:171]\n"
    "v_pk_mul_f32 v[172:173], v[172:173], v[174:175]\n"
    "v_pk_mul_f32 v[208:209], v[208:209], v[164:165]\n"
    "v_pk_mul_f32 v[212:213], v[212:213], v[172:173]\n"
    "v_pk_mul_f32 v[216:217], v[160:161], v[164:165]\n"
    "v_pk_mul_f32 v[218:219], v[168:169], v[172:173]\n"
    "v_pk_fma_f32 v[208:209], v[210:211], v[160:161], v[208:209]\n"
    "v_pk_fma_f32 v[212:213], v[214:215], v[168:169], v[212:213]\n"
    "v_mfma_f32_32x32x16_bf16 v[160:175], v[64:67], a[32:35], v[128:143]\n"
    "s_waitcnt vmcnt(8) lgkmcnt(0)\n"
    "s_barrier\n"
    "ds_read_b128 a[32:35], v226 offset:24576\n"
    "v_add_f32 v224, v220, v221\n"
    "v_rcp_f32 v216, v216\n"
    "v_mfma_f32_32x32x16_bf16 v[160:175], v[68:71], a[36:39], v[160:175]\n"
    "ds_read_b128 a[36:39], v227 offset:24576\n"
    "ds_read_b128 a[64:67], v236 offset:64\n"
    "v_cvt_pk_bf16_f32 v224, v224, v224\n"
    "v_rcp_f32 v217, v217\n"
    "v_rcp_f32 v218, v218\n"
    "v_mfma_f32_32x32x16_bf16 v[160:175], v[72:75], a[40:43], v[160:175]\n"
    "ds_read_b128 a[40:43], v226 offset:25088\n"
    "ds_read_b64_tr_b16 a[68:69], v228 offset:14336\n"
    "v_rcp_f32 v219, v219\n"
    "v_exp_f32_e64 v176, v176 clamp\n"
    "v_mfma_f32_32x32x16_bf16 v[160:175], v[76:79], a[44:47], v[160:175]\n"
    "ds_read_b128 a[44:47], v227 offset:25088\n"
    "ds_read_b64_tr_b16 a[70:71], v229 offset:14336\n"
    "v_exp_f32_e64 v177, v177 clamp\n"
    "v_exp_f32_e64 v178, v178 clamp\n"
    "v_mfma_f32_32x32x16_bf16 v[160:175], v[80:83], a[48:51], v[160:175]\n"
    "ds_read_b128 a[48:51], v226 offset:25600\n"
    "ds_read_b64_tr_b16 a[72:73], v230 offset:14336\n"
    "v_exp_f32_e64 v179, v179 clamp\n"
    "v_exp_f32_e64 v180, v180 clamp\n"
    "v_mfma_f32_32x32x16_bf16 v[160:175], v[84:87], a[52:55], v[160:175]\n"
    "ds_read_b128 a[52:55], v227 offset:25600\n"
    "ds_read_b64_tr_b16 a[74:75], v231 offset:14336\n"
    "v_exp_f32_e64 v181, v181 clamp\n"
    "v_exp_f32_e64 v182, v182 clamp\n"
    "v_mfma_f32_32x32x16_bf16 v[160:175], v[88:91], a[56:59], v[160:175]\n"
    "ds_read_b128 a[56:59], v226 offset:26112\n"
    "ds_write_b16 v233, v224 offset:41024\n"
    "v_exp_f32_e64 v183, v183 clamp\n"
    "v_exp_f32_e64 v184, v184 clamp\n"
    "v_mfma_f32_32x32x16_bf16 v[160:175], v[92:95], a[60:63], v[160:175]\n"
    "s_waitcnt lgkmcnt(2)\n"
    "ds_read_b128 a[60:63], v227 offset:26112\n"
    "v_exp_f32_e64 v185, v185 clamp\n"
    "v_exp_f32_e64 v186, v186 clamp\n"
    "v_exp_f32_e64 v187, v187 clamp\n"
    "v_mfma_f32_16x16x32_bf16 a[84:87], a[64:67], a[68:71], a[84:87]\n"
    "v_exp_f32_e64 v188, v188 clamp\n"
    "v_exp_f32_e64 v189, v189 clamp\n"
    "v_mfma_f32_16x16x32_bf16 a[88:91], a[64:67], a[72:75], a[88:91]\n"
    "v_exp_f32_e64 v190, v190 clamp\n"
    "v_exp_f32_e64 v191, v191 clamp\n"
    "v_pk_fma_f32 v[220:221], v[208:209], v[216:217], v[222:223]\n"
    "v_pk_add_f32 v[176:177], v[176:177], v[192:193]\n"
    "v_pk_add_f32 v[178:179], v[178:179], v[194:195]\n"
    "v_pk_add_f32 v[180:181], v[180:181], v[196:197]\n"
    "v_pk_add_f32 v[182:183], v[182:183], v[198:199]\n"
    "v_pk_fma_f32 v[220:221], v[212:213], v[218:219], v[220:221]\n"
    "v_pk_add_f32 v[184:185], v[184:185], v[200:201]\n"
    "v_pk_add_f32 v[186:187], v[186:187], v[202:203]\n"
    "v_pk_add_f32 v[188:189], v[188:189], v[204:205]\n"
    "v_pk_add_f32 v[190:191], v[190:191], v[206:207]\n"
    "v_pk_add_f32 v[208:209], v[176:177], v[178:179]\n"
    "v_pk_add_f32 v[210:211], v[180:181], v[182:183]\n"
    "v_pk_add_f32 v[212:213], v[184:185], v[186:187]\n"
    "v_pk_add_f32 v[214:215], v[188:189], v[190:191]\n"
    "v_pk_mul_f32 v[176:177], v[176:177], v[178:179]\n"
    "v_pk_mul_f32 v[180:181], v[180:181], v[182:183]\n"
    "v_pk_mul_f32 v[184:185], v[184:185], v[186:187]\n"
    "v_pk_mul_f32 v[188:189], v[188:189], v[190:191]\n"
    "v_pk_mul_f32 v[208:209], v[208:209], v[180:181]\n"
    "v_pk_mul_f32 v[212:213], v[212:213], v[188:189]\n"
    "v_pk_mul_f32 v[216:217], v[176:177], v[180:181]\n"
    "v_pk_mul_f32 v[218:219], v[184:185], v[188:189]\n"
    "v_pk_fma_f32 v[208:209], v[210:211], v[176:177], v[208:209]\n"
    "v_pk_fma_f32 v[212:213], v[214:215], v[184:185], v[212:213]\n"
    "v_mfma_f32_32x32x16_bf16 v[176:191], a[0:3], a[32:35], v[144:159]\n"
    "s_waitcnt vmcnt(6) lgkmcnt(0)\n"
    "s_barrier\n"
    "ds_read_b128 a[32:35], v226 offset:26624\n"
    "v_add_f32 v224, v220, v221\n"
    "v_rcp_f32 v216, v216\n"
    "v_mfma_f32_32x32x16_bf16 v[176:191], a[4:7], a[36:39], v[176:191]\n"
    "ds_read_b128 a[36:39], v227 offset:26624\n"
    "ds_read_b128 a[64:67], v237 offset:64\n"
    "v_cvt_pk_bf16_f32 v224, v224, v224\n"
    "v_rcp_f32 v217, v217\n"
    "v_rcp_f32 v218, v218\n"
    "v_mfma_f32_32x32x16_bf16 v[176:191], a[8:11], a[40:43], v[176:191]\n"
    "ds_read_b128 a[40:43], v226 offset:27136\n"
    "ds_read_b64_tr_b16 a[68:69], v228 offset:16384\n"
    "v_rcp_f32 v219, v219\n"
    "v_exp_f32_e64 v160, v160 clamp\n"
    "v_mfma_f32_32x32x16_bf16 v[176:191], a[12:15], a[44:47], v[176:191]\n"
    "ds_read_b128 a[44:47], v227 offset:27136\n"
    "ds_read_b64_tr_b16 a[70:71], v229 offset:16384\n"
    "v_exp_f32_e64 v161, v161 clamp\n"
    "v_exp_f32_e64 v162, v162 clamp\n"
    "v_mfma_f32_32x32x16_bf16 v[176:191], a[16:19], a[48:51], v[176:191]\n"
    "ds_read_b128 a[48:51], v226 offset:27648\n"
    "ds_read_b64_tr_b16 a[72:73], v230 offset:16384\n"
    "v_exp_f32_e64 v163, v163 clamp\n"
    "v_exp_f32_e64 v164, v164 clamp\n"
    "v_mfma_f32_32x32x16_bf16 v[176:191], a[20:23], a[52:55], v[176:191]\n"
    "ds_read_b128 a[52:55], v227 offset:27648\n"
    "ds_read_b64_tr_b16 a[74:75], v231 offset:16384\n"
    "v_exp_f32_e64 v165, v165 clamp\n"
    "v_exp_f32_e64 v166, v166 clamp\n"
    "v_mfma_f32_32x32x16_bf16 v[176:191], a[24:27], a[56:59], v[176:191]\n"
    "ds_read_b128 a[56:59], v226 offset:28160\n"
    "ds_write_b16 v232, v224 offset:128\n"
    "v_exp_f32_e64 v167, v167 clamp\n"
    "v_exp_f32_e64 v168, v168 clamp\n"
    "v_mfma_f32_32x32x16_bf16 v[176:191], a[28:31], a[60:63], v[176:191]\n"
    "s_waitcnt lgkmcnt(2)\n"
    "ds_read_b128 a[60:63], v227 offset:28160\n"
    "v_exp_f32_e64 v169, v169 clamp\n"
    "v_exp_f32_e64 v170, v170 clamp\n"
    "v_exp_f32_e64 v171, v171 clamp\n"
    "v_mfma_f32_16x16x32_bf16 a[84:87], a[64:67], a[68:71], a[84:87]\n"
    "v_exp_f32_e64 v172, v172 clamp\n"
    "v_exp_f32_e64 v173, v173 clamp\n"
    "v_mfma_f32_16x16x32_bf16 a[88:91], a[64:67], a[72:75], a[88:91]\n"
    "v_exp_f32_e64 v174, v174 clamp\n"
    "v_exp_f32_e64 v175, v175 clamp\n"
    "v_pk_fma_f32 v[220:221], v[208:209], v[216:217], v[222:223]\n"
    "v_pk_add_f32 v[160:161], v[160:161], v[192:193]\n"
    "v_pk_add_f32 v[162:163], v[162:163], v[194:195]\n"
    "v_pk_add_f32 v[164:165], v[164:165], v[196:197]\n"
    "v_pk_add_f32 v[166:167], v[166:167], v[198:199]\n"
    "v_pk_fma_f32 v[220:221], v[212:213], v[218:219], v[220:221]\n"
    "v_pk_add_f32 v[168:169], v[168:169], v[200:201]\n"
    "v_pk_add_f32 v[170:171], v[170:171], v[202:203]\n"
    "v_pk_add_f32 v[172:173], v[172:173], v[204:205]\n"
    "v_pk_add_f32 v[174:175], v[174:175], v[206:207]\n"
    "v_pk_add_f32 v[208:209], v[160:161], v[162:163]\n"
    "v_pk_add_f32 v[210:211], v[164:165], v[166:167]\n"
    "v_pk_add_f32 v[212:213], v[168:169], v[170:171]\n"
    "v_pk_add_f32 v[214:215], v[172:173], v[174:175]\n"
    "v_pk_mul_f32 v[160:161], v[160:161], v[162:163]\n"
    "v_pk_mul_f32 v[164:165], v[164:165], v[166:167]\n"
    "v_pk_mul_f32 v[168:169], v[168:169], v[170:171]\n"
    "v_pk_mul_f32 v[172:173], v[172:173], v[174:175]\n"
    "v_pk_mul_f32 v[208:209], v[208:209], v[164:165]\n"
    "v_pk_mul_f32 v[212:213], v[212:213], v[172:173]\n"
    "v_pk_mul_f32 v[216:217], v[160:161], v[164:165]\n"
    "v_pk_mul_f32 v[218:219], v[168:169], v[172:173]\n"
    "v_pk_fma_f32 v[208:209], v[210:211], v[160:161], v[208:209]\n"
    "v_pk_fma_f32 v[212:213], v[214:215], v[168:169], v[212:213]\n"
    "v_mfma_f32_32x32x16_bf16 v[160:175], v[0:3], a[32:35], v[96:111]\n"
    "s_waitcnt vmcnt(4) lgkmcnt(0)\n"
    "s_barrier\n"
    "ds_read_b128 a[32:35], v226 offset:28672\n"
    "v_add_f32 v224, v220, v221\n"
    "v_rcp_f32 v216, v216\n"
    "v_mfma_f32_32x32x16_bf16 v[160:175], v[4:7], a[36:39], v[160:175]\n"
    "ds_read_b128 a[36:39], v227 offset:28672\n"
    "ds_read_b128 a[64:67], v234 offset:128\n"
    "v_cvt_pk_bf16_f32 v224, v224, v224\n"
    "v_rcp_f32 v217, v217\n"
    "v_rcp_f32 v218, v218\n"
    "v_mfma_f32_32x32x16_bf16 v[160:175], v[8:11], a[40:43], v[160:175]\n"
    "ds_read_b128 a[40:43], v226 offset:29184\n"
    "ds_read_b64_tr_b16 a[68:69], v228 offset:18432\n"
    "v_rcp_f32 v219, v219\n"
    "v_exp_f32_e64 v176, v176 clamp\n"
    "v_mfma_f32_32x32x16_bf16 v[160:175], v[12:15], a[44:47], v[160:175]\n"
    "ds_read_b128 a[44:47], v227 offset:29184\n"
    "ds_read_b64_tr_b16 a[70:71], v229 offset:18432\n"
    "v_exp_f32_e64 v177, v177 clamp\n"
    "v_exp_f32_e64 v178, v178 clamp\n"
    "v_mfma_f32_32x32x16_bf16 v[160:175], v[16:19], a[48:51], v[160:175]\n"
    "ds_read_b128 a[48:51], v226 offset:29696\n"
    "ds_read_b64_tr_b16 a[72:73], v230 offset:18432\n"
    "v_exp_f32_e64 v179, v179 clamp\n"
    "v_exp_f32_e64 v180, v180 clamp\n"
    "v_mfma_f32_32x32x16_bf16 v[160:175], v[20:23], a[52:55], v[160:175]\n"
    "ds_read_b128 a[52:55], v227 offset:29696\n"
    "ds_read_b64_tr_b16 a[74:75], v231 offset:18432\n"
    "v_exp_f32_e64 v181, v181 clamp\n"
    "v_exp_f32_e64 v182, v182 clamp\n"
    "v_mfma_f32_32x32x16_bf16 v[160:175], v[24:27], a[56:59], v[160:175]\n"
    "ds_read_b128 a[56:59], v226 offset:30208\n"
    "ds_write_b16 v232, v224 offset:41088\n"
    "v_exp_f32_e64 v183, v183 clamp\n"
    "v_exp_f32_e64 v184, v184 clamp\n"
    "v_mfma_f32_32x32x16_bf16 v[160:175], v[28:31], a[60:63], v[160:175]\n"
    "s_waitcnt lgkmcnt(2)\n"
    "ds_read_b128 a[60:63], v227 offset:30208\n"
    "v_exp_f32_e64 v185, v185 clamp\n"
    "v_exp_f32_e64 v186, v186 clamp\n"
    "v_exp_f32_e64 v187, v187 clamp\n"
    "v_mfma_f32_16x16x32_bf16 a[76:79], a[64:67], a[68:71], a[76:79]\n"
    "v_exp_f32_e64 v188, v188 clamp\n"
    "v_exp_f32_e64 v189, v189 clamp\n"
    "v_mfma_f32_16x16x32_bf16 a[80:83], a[64:67], a[72:75], a[80:83]\n"
    "v_exp_f32_e64 v190, v190 clamp\n"
    "v_exp_f32_e64 v191, v191 clamp\n"
    "v_pk_fma_f32 v[220:221], v[208:209], v[216:217], v[222:223]\n"
    "v_pk_add_f32 v[176:177], v[176:177], v[192:193]\n"
    "v_pk_add_f32 v[178:179], v[178:179], v[194:195]\n"
    "v_pk_add_f32 v[180:181], v[180:181], v[196:197]\n"
    "v_pk_add_f32 v[182:183], v[182:183], v[198:199]\n"
    "v_pk_fma_f32 v[220:221], v[212:213], v[218:219], v[220:221]\n"
    "v_pk_add_f32 v[184:185], v[184:185], v[200:201]\n"
    "v_pk_add_f32 v[186:187], v[186:187], v[202:203]\n"
    "v_pk_add_f32 v[188:189], v[188:189], v[204:205]\n"
    "v_pk_add_f32 v[190:191], v[190:191], v[206:207]\n"
    "v_pk_add_f32 v[208:209], v[176:177], v[178:179]\n"
    "v_pk_add_f32 v[210:211], v[180:181], v[182:183]\n"
    "v_pk_add_f32 v[212:213], v[184:185], v[186:187]\n"
    "v_pk_add_f32 v[214:215], v[188:189], v[190:191]\n"
    "v_pk_mul_f32 v[176:177], v[176:177], v[178:179]\n"
    "v_pk_mul_f32 v[180:181], v[180:181], v[182:183]\n"
    "v_pk_mul_f32 v[184:185], v[184:185], v[186:187]\n"
    "v_pk_mul_f32 v[188:189], v[188:189], v[190:191]\n"
    "v_pk_mul_f32 v[208:209], v[208:209], v[180:181]\n"
    "v_pk_mul_f32 v[212:213], v[212:213], v[188:189]\n"
    "v_pk_mul_f32 v[216:217], v[176:177], v[180:181]\n"
    "v_pk_mul_f32 v[218:219], v[184:185], v[188:189]\n"
    "v_pk_fma_f32 v[208:209], v[210:211], v[176:177], v[208:209]\n"
    "v_pk_fma_f32 v[212:213], v[214:215], v[184:185], v[212:213]\n"
    "v_mfma_f32_32x32x16_bf16 v[176:191], v[32:35], a[32:35], v[112:127]\n"
    "s_waitcnt vmcnt(2) lgkmcnt(0)\n"
    "s_barrier\n"
    "ds_read_b128 a[32:35], v226 offset:30720\n"
    "v_add_f32 v224, v220, v221\n"
    "v_rcp_f32 v216, v216\n"
    "v_mfma_f32_32x32x16_bf16 v[176:191], v[36:39], a[36:39], v[176:191]\n"
    "ds_read_b128 a[36:39], v227 offset:30720\n"
    "ds_read_b128 a[64:67], v235 offset:128\n"
    "v_cvt_pk_bf16_f32 v224, v224, v224\n"
    "v_rcp_f32 v217, v217\n"
    "v_rcp_f32 v218, v218\n"
    "v_mfma_f32_32x32x16_bf16 v[176:191], v[40:43], a[40:43], v[176:191]\n"
    "ds_read_b128 a[40:43], v226 offset:31232\n"
    "ds_read_b64_tr_b16 a[68:69], v228 offset:20480\n"
    "v_rcp_f32 v219, v219\n"
    "v_exp_f32_e64 v160, v160 clamp\n"
    "v_mfma_f32_32x32x16_bf16 v[176:191], v[44:47], a[44:47], v[176:191]\n"
    "ds_read_b128 a[44:47], v227 offset:31232\n"
    "ds_read_b64_tr_b16 a[70:71], v229 offset:20480\n"
    "v_exp_f32_e64 v161, v161 clamp\n"
    "v_exp_f32_e64 v162, v162 clamp\n"
    "v_mfma_f32_32x32x16_bf16 v[176:191], v[48:51], a[48:51], v[176:191]\n"
    "ds_read_b128 a[48:51], v226 offset:31744\n"
    "ds_read_b64_tr_b16 a[72:73], v230 offset:20480\n"
    "v_exp_f32_e64 v163, v163 clamp\n"
    "v_exp_f32_e64 v164, v164 clamp\n"
    "v_mfma_f32_32x32x16_bf16 v[176:191], v[52:55], a[52:55], v[176:191]\n"
    "ds_read_b128 a[52:55], v227 offset:31744\n"
    "ds_read_b64_tr_b16 a[74:75], v231 offset:20480\n"
    "v_exp_f32_e64 v165, v165 clamp\n"
    "v_exp_f32_e64 v166, v166 clamp\n"
    "v_mfma_f32_32x32x16_bf16 v[176:191], v[56:59], a[56:59], v[176:191]\n"
    "ds_read_b128 a[56:59], v226 offset:32256\n"
    "ds_write_b16 v233, v224 offset:128\n"
    "v_exp_f32_e64 v167, v167 clamp\n"
    "v_exp_f32_e64 v168, v168 clamp\n"
    "v_mfma_f32_32x32x16_bf16 v[176:191], v[60:63], a[60:63], v[176:191]\n"
    "s_waitcnt lgkmcnt(2)\n"
    "ds_read_b128 a[60:63], v227 offset:32256\n"
    "v_exp_f32_e64 v169, v169 clamp\n"
    "v_exp_f32_e64 v170, v170 clamp\n"
    "v_exp_f32_e64 v171, v171 clamp\n"
    "v_mfma_f32_16x16x32_bf16 a[76:79], a[64:67], a[68:71], a[76:79]\n"
    "v_exp_f32_e64 v172, v172 clamp\n"
    "v_exp_f32_e64 v173, v173 clamp\n"
    "v_mfma_f32_16x16x32_bf16 a[80:83], a[64:67], a[72:75], a[80:83]\n"
    "v_exp_f32_e64 v174, v174 clamp\n"
    "v_exp_f32_e64 v175, v175 clamp\n"
    "v_pk_fma_f32 v[220:221], v[208:209], v[216:217], v[222:223]\n"
    "v_pk_add_f32 v[160:161], v[160:161], v[192:193]\n"
    "v_pk_add_f32 v[162:163], v[162:163], v[194:195]\n"
    "v_pk_add_f32 v[164:165], v[164:165], v[196:197]\n"
    "v_pk_add_f32 v[166:167], v[166:167], v[198:199]\n"
    "v_pk_fma_f32 v[220:221], v[212:213], v[218:219], v[220:221]\n"
    "v_pk_add_f32 v[168:169], v[168:169], v[200:201]\n"
    "v_pk_add_f32 v[170:171], v[170:171], v[202:203]\n"
    "v_pk_add_f32 v[172:173], v[172:173], v[204:205]\n"
    "v_pk_add_f32 v[174:175], v[174:175], v[206:207]\n"
    "v_pk_add_f32 v[208:209], v[160:161], v[162:163]\n"
    "v_pk_add_f32 v[210:211], v[164:165], v[166:167]\n"
    "v_pk_add_f32 v[212:213], v[168:169], v[170:171]\n"
    "v_pk_add_f32 v[214:215], v[172:173], v[174:175]\n"
    "v_pk_mul_f32 v[160:161], v[160:161], v[162:163]\n"
    "v_pk_mul_f32 v[164:165], v[164:165], v[166:167]\n"
    "v_pk_mul_f32 v[168:169], v[168:169], v[170:171]\n"
    "v_pk_mul_f32 v[172:173], v[172:173], v[174:175]\n"
    "v_pk_mul_f32 v[208:209], v[208:209], v[164:165]\n"
    "v_pk_mul_f32 v[212:213], v[212:213], v[172:173]\n"
    "v_pk_mul_f32 v[216:217], v[160:161], v[164:165]\n"
    "v_pk_mul_f32 v[218:219], v[168:169], v[172:173]\n"
    "v_pk_fma_f32 v[208:209], v[210:211], v[160:161], v[208:209]\n"
    "v_pk_fma_f32 v[212:213], v[214:215], v[168:169], v[212:213]\n"
    "v_mfma_f32_32x32x16_bf16 v[160:175], v[64:67], a[32:35], v[128:143]\n"
    "s_waitcnt vmcnt(0) lgkmcnt(0)\n"
    "s_barrier\n"
    "ds_read_b128 a[32:35], v226 offset:6144\n"
    "v_add_f32 v224, v220, v221\n"
    "v_rcp_f32 v216, v216\n"
    "v_mfma_f32_32x32x16_bf16 v[160:175], v[68:71], a[36:39], v[160:175]\n"
    "ds_read_b128 a[36:39], v227 offset:6144\n"
    "ds_read_b128 a[64:67], v236 offset:128\n"
    "v_cvt_pk_bf16_f32 v224, v224, v224\n"
    "v_rcp_f32 v217, v217\n"
    "v_rcp_f32 v218, v218\n"
    "v_mfma_f32_32x32x16_bf16 v[160:175], v[72:75], a[40:43], v[160:175]\n"
    "ds_read_b128 a[40:43], v226 offset:6656\n"
    "ds_read_b64_tr_b16 a[68:69], v228 offset:22528\n"
    "v_rcp_f32 v219, v219\n"
    "v_exp_f32_e64 v176, v176 clamp\n"
    "v_mfma_f32_32x32x16_bf16 v[160:175], v[76:79], a[44:47], v[160:175]\n"
    "ds_read_b128 a[44:47], v227 offset:6656\n"
    "ds_read_b64_tr_b16 a[70:71], v229 offset:22528\n"
    "v_exp_f32_e64 v177, v177 clamp\n"
    "v_exp_f32_e64 v178, v178 clamp\n"
    "v_mfma_f32_32x32x16_bf16 v[160:175], v[80:83], a[48:51], v[160:175]\n"
    "ds_read_b128 a[48:51], v226 offset:7168\n"
    "ds_read_b64_tr_b16 a[72:73], v230 offset:22528\n"
    "v_exp_f32_e64 v179, v179 clamp\n"
    "v_exp_f32_e64 v180, v180 clamp\n"
    "v_mfma_f32_32x32x16_bf16 v[160:175], v[84:87], a[52:55], v[160:175]\n"
    "ds_read_b128 a[52:55], v227 offset:7168\n"
    "ds_read_b64_tr_b16 a[74:75], v231 offset:22528\n"
    "v_exp_f32_e64 v181, v181 clamp\n"
    "v_exp_f32_e64 v182, v182 clamp\n"
    "v_mfma_f32_32x32x16_bf16 v[160:175], v[88:91], a[56:59], v[160:175]\n"
    "ds_read_b128 a[56:59], v226 offset:7680\n"
    "ds_write_b16 v233, v224 offset:41088\n"
    "v_exp_f32_e64 v183, v183 clamp\n"
    "v_exp_f32_e64 v184, v184 clamp\n"
    "v_mfma_f32_32x32x16_bf16 v[160:175], v[92:95], a[60:63], v[160:175]\n"
    "s_waitcnt lgkmcnt(2)\n"
    "ds_read_b128 a[60:63], v227 offset:7680\n"
    "v_exp_f32_e64 v185, v185 clamp\n"
    "v_exp_f32_e64 v186, v186 clamp\n"
    "v_exp_f32_e64 v187, v187 clamp\n"
    "v_mfma_f32_16x16x32_bf16 a[84:87], a[64:67], a[68:71], a[84:87]\n"
    "v_exp_f32_e64 v188, v188 clamp\n"
    "v_exp_f32_e64 v189, v189 clamp\n"
    "v_mfma_f32_16x16x32_bf16 a[88:91], a[64:67], a[72:75], a[88:91]\n"
    "v_exp_f32_e64 v190, v190 clamp\n"
    "v_exp_f32_e64 v191, v191 clamp\n"
    "v_pk_fma_f32 v[220:221], v[208:209], v[216:217], v[222:223]\n"
    "v_pk_add_f32 v[176:177], v[176:177], v[192:193]\n"
    "v_pk_add_f32 v[178:179], v[178:179], v[194:195]\n"
    "v_pk_add_f32 v[180:181], v[180:181], v[196:197]\n"
    "v_pk_add_f32 v[182:183], v[182:183], v[198:199]\n"
    "v_pk_fma_f32 v[220:221], v[212:213], v[218:219], v[220:221]\n"
    "v_pk_add_f32 v[184:185], v[184:185], v[200:201]\n"
    "v_pk_add_f32 v[186:187], v[186:187], v[202:203]\n"
    "v_pk_add_f32 v[188:189], v[188:189], v[204:205]\n"
    "v_pk_add_f32 v[190:191], v[190:191], v[206:207]\n"
    "v_pk_add_f32 v[208:209], v[176:177], v[178:179]\n"
    "v_pk_add_f32 v[210:211], v[180:181], v[182:183]\n"
    "v_pk_add_f32 v[212:213], v[184:185], v[186:187]\n"
    "v_pk_add_f32 v[214:215], v[188:189], v[190:191]\n"
    "v_pk_mul_f32 v[176:177], v[176:177], v[178:179]\n"
    "v_pk_mul_f32 v[180:181], v[180:181], v[182:183]\n"
    "v_pk_mul_f32 v[184:185], v[184:185], v[186:187]\n"
    "v_pk_mul_f32 v[188:189], v[188:189], v[190:191]\n"
    "v_pk_mul_f32 v[208:209], v[208:209], v[180:181]\n"
    "v_pk_mul_f32 v[212:213], v[212:213], v[188:189]\n"
    "v_pk_mul_f32 v[216:217], v[176:177], v[180:181]\n"
    "v_pk_mul_f32 v[218:219], v[184:185], v[188:189]\n"
    "v_pk_fma_f32 v[208:209], v[210:211], v[176:177], v[208:209]\n"
    "v_pk_fma_f32 v[212:213], v[214:215], v[184:185], v[212:213]\n"
    "v_mfma_f32_32x32x16_bf16 v[176:191], a[0:3], a[32:35], v[144:159]\n"
    "s_waitcnt lgkmcnt(0)\n"
    "s_barrier\n"
    "v_add_f32 v224, v220, v221\n"
    "v_rcp_f32 v216, v216\n"
    "v_mfma_f32_32x32x16_bf16 v[176:191], a[4:7], a[36:39], v[176:191]\n"
    "ds_read_b128 a[64:67], v237 offset:128\n"
    "v_cvt_pk_bf16_f32 v224, v224, v224\n"
    "v_rcp_f32 v217, v217\n"
    "v_rcp_f32 v218, v218\n"
    "v_mfma_f32_32x32x16_bf16 v[176:191], a[8:11], a[40:43], v[176:191]\n"
    "ds_read_b64_tr_b16 a[68:69], v228 offset:24576\n"
    "v_rcp_f32 v219, v219\n"
    "v_exp_f32_e64 v160, v160 clamp\n"
    "v_mfma_f32_32x32x16_bf16 v[176:191], a[12:15], a[44:47], v[176:191]\n"
    "ds_read_b64_tr_b16 a[70:71], v229 offset:24576\n"
    "v_exp_f32_e64 v161, v161 clamp\n"
    "v_exp_f32_e64 v162, v162 clamp\n"
    "v_mfma_f32_32x32x16_bf16 v[176:191], a[16:19], a[48:51], v[176:191]\n"
    "ds_read_b64_tr_b16 a[72:73], v230 offset:24576\n"
    "v_exp_f32_e64 v163, v163 clamp\n"
    "v_exp_f32_e64 v164, v164 clamp\n"
    "v_mfma_f32_32x32x16_bf16 v[176:191], a[20:23], a[52:55], v[176:191]\n"
    "ds_read_b64_tr_b16 a[74:75], v231 offset:24576\n"
    "v_exp_f32_e64 v165, v165 clamp\n"
    "v_exp_f32_e64 v166, v166 clamp\n"
    "v_mfma_f32_32x32x16_bf16 v[176:191], a[24:27], a[56:59], v[176:191]\n"
    "ds_write_b16 v232, v224 offset:192\n"
    "v_exp_f32_e64 v167, v167 clamp\n"
    "v_exp_f32_e64 v168, v168 clamp\n"
    "v_mfma_f32_32x32x16_bf16 v[176:191], a[28:31], a[60:63], v[176:191]\n"
    "s_waitcnt lgkmcnt(1)\n"
    "v_exp_f32_e64 v169, v169 clamp\n"
    "v_exp_f32_e64 v170, v170 clamp\n"
    "v_exp_f32_e64 v171, v171 clamp\n"
    "v_mfma_f32_16x16x32_bf16 a[84:87], a[64:67], a[68:71], a[84:87]\n"
    "v_exp_f32_e64 v172, v172 clamp\n"
    "v_exp_f32_e64 v173, v173 clamp\n"
    "v_mfma_f32_16x16x32_bf16 a[88:91], a[64:67], a[72:75], a[88:91]\n"
    "v_exp_f32_e64 v174, v174 clamp\n"
    "v_exp_f32_e64 v175, v175 clamp\n"
    "v_pk_fma_f32 v[220:221], v[208:209], v[216:217], v[222:223]\n"
    "v_pk_add_f32 v[160:161], v[160:161], v[192:193]\n"
    "v_pk_add_f32 v[162:163], v[162:163], v[194:195]\n"
    "v_pk_add_f32 v[164:165], v[164:165], v[196:197]\n"
    "v_pk_add_f32 v[166:167], v[166:167], v[198:199]\n"
    "v_pk_fma_f32 v[220:221], v[212:213], v[218:219], v[220:221]\n"
    "v_pk_add_f32 v[168:169], v[168:169], v[200:201]\n"
    "v_pk_add_f32 v[170:171], v[170:171], v[202:203]\n"
    "v_pk_add_f32 v[172:173], v[172:173], v[204:205]\n"
    "v_pk_add_f32 v[174:175], v[174:175], v[206:207]\n"
    "v_pk_add_f32 v[208:209], v[160:161], v[162:163]\n"
    "v_pk_add_f32 v[210:211], v[164:165], v[166:167]\n"
    "v_pk_add_f32 v[212:213], v[168:169], v[170:171]\n"
    "v_pk_add_f32 v[214:215], v[172:173], v[174:175]\n"
    "v_pk_mul_f32 v[160:161], v[160:161], v[162:163]\n"
    "v_pk_mul_f32 v[164:165], v[164:165], v[166:167]\n"
    "v_pk_mul_f32 v[168:169], v[168:169], v[170:171]\n"
    "v_pk_mul_f32 v[172:173], v[172:173], v[174:175]\n"
    "v_pk_mul_f32 v[208:209], v[208:209], v[164:165]\n"
    "v_pk_mul_f32 v[212:213], v[212:213], v[172:173]\n"
    "v_pk_mul_f32 v[216:217], v[160:161], v[164:165]\n"
    "v_pk_mul_f32 v[218:219], v[168:169], v[172:173]\n"
    "v_pk_fma_f32 v[208:209], v[210:211], v[160:161], v[208:209]\n"
    "v_pk_fma_f32 v[212:213], v[214:215], v[168:169], v[212:213]\n"
    "v_rcp_f32 v216, v216\n"
    "v_rcp_f32 v217, v217\n"
    "v_rcp_f32 v218, v218\n"
    "v_rcp_f32 v219, v219\n"
    "v_add_f32 v224, v220, v221\n"
    "v_cvt_pk_bf16_f32 v224, v224, v224\n"
    "ds_write_b16 v232, v224 offset:41152\n"
    "ds_read_b64_tr_b16 a[36:37], v228 offset:26624\n"
    "ds_read_b64_tr_b16 a[38:39], v229 offset:26624\n"
    "v_exp_f32_e64 v176, v176 clamp\n"
    "v_exp_f32_e64 v177, v177 clamp\n"
    "v_exp_f32_e64 v178, v178 clamp\n"
    "v_exp_f32_e64 v179, v179 clamp\n"
    "v_exp_f32_e64 v180, v180 clamp\n"
    "v_exp_f32_e64 v181, v181 clamp\n"
    "v_exp_f32_e64 v182, v182 clamp\n"
    "v_exp_f32_e64 v183, v183 clamp\n"
    "v_exp_f32_e64 v184, v184 clamp\n"
    "v_exp_f32_e64 v185, v185 clamp\n"
    "v_exp_f32_e64 v186, v186 clamp\n"
    "v_exp_f32_e64 v187, v187 clamp\n"
    "v_exp_f32_e64 v188, v188 clamp\n"
    "v_exp_f32_e64 v189, v189 clamp\n"
    "v_exp_f32_e64 v190, v190 clamp\n"
    "v_exp_f32_e64 v191, v191 clamp\n"
    "v_pk_fma_f32 v[220:221], v[208:209], v[216:217], v[222:223]\n"
    "v_pk_add_f32 v[176:177], v[176:177], v[192:193]\n"
    "v_pk_add_f32 v[178:179], v[178:179], v[194:195]\n"
    "v_pk_add_f32 v[180:181], v[180:181], v[196:197]\n"
    "v_pk_add_f32 v[182:183], v[182:183], v[198:199]\n"
    "v_pk_fma_f32 v[220:221], v[212:213], v[218:219], v[220:221]\n"
    "v_pk_add_f32 v[184:185], v[184:185], v[200:201]\n"
    "v_pk_add_f32 v[186:187], v[186:187], v[202:203]\n"
    "v_pk_add_f32 v[188:189], v[188:189], v[204:205]\n"
    "v_pk_add_f32 v[190:191], v[190:191], v[206:207]\n"
    "v_pk_add_f32 v[208:209], v[176:177], v[178:179]\n"
    "v_pk_add_f32 v[210:211], v[180:181], v[182:183]\n"
    "v_pk_add_f32 v[212:213], v[184:185], v[186:187]\n"
    "v_pk_add_f32 v[214:215], v[188:189], v[190:191]\n"
    "v_pk_mul_f32 v[176:177], v[176:177], v[178:179]\n"
    "v_pk_mul_f32 v[180:181], v[180:181], v[182:183]\n"
    "v_pk_mul_f32 v[184:185], v[184:185], v[186:187]\n"
    "v_pk_mul_f32 v[188:189], v[188:189], v[190:191]\n"
    "v_pk_mul_f32 v[208:209], v[208:209], v[180:181]\n"
    "v_pk_mul_f32 v[212:213], v[212:213], v[188:189]\n"
    "v_pk_mul_f32 v[216:217], v[176:177], v[180:181]\n"
    "v_pk_mul_f32 v[218:219], v[184:185], v[188:189]\n"
    "v_pk_fma_f32 v[208:209], v[210:211], v[176:177], v[208:209]\n"
    "v_pk_fma_f32 v[212:213], v[214:215], v[184:185], v[212:213]\n"
    "v_rcp_f32 v216, v216\n"
    "v_rcp_f32 v217, v217\n"
    "v_add_f32 v224, v220, v221\n"
    "v_cvt_pk_bf16_f32 v224, v224, v224\n"
    "v_rcp_f32 v218, v218\n"
    "v_rcp_f32 v219, v219\n"
    "ds_write_b16 v233, v224 offset:192\n"
    "ds_read_b64_tr_b16 a[40:41], v230 offset:26624\n"
    "ds_read_b64_tr_b16 a[42:43], v231 offset:26624\n"
    "v_pk_fma_f32 v[220:221], v[208:209], v[216:217], v[222:223]\n"
    "s_nop 0\n"
    "v_pk_fma_f32 v[220:221], v[212:213], v[218:219], v[220:221]\n"
    "v_add_f32 v224, v220, v221\n"
    "v_cvt_pk_bf16_f32 v224, v224, v224\n"
    "ds_write_b16 v233, v224 offset:41152\n"
    "ds_read_b64_tr_b16 a[48:49], v228 offset:28672\n"
    "ds_read_b64_tr_b16 a[50:51], v229 offset:28672\n"
    "ds_read_b64_tr_b16 a[52:53], v230 offset:28672\n"
    "ds_read_b64_tr_b16 a[54:55], v231 offset:28672\n"
    "ds_read_b64_tr_b16 a[128:129], v228 offset:30720\n"
    "ds_read_b64_tr_b16 a[130:131], v229 offset:30720\n"
    "ds_read_b64_tr_b16 a[132:133], v230 offset:30720\n"
    "ds_read_b64_tr_b16 a[134:135], v231 offset:30720\n"
    "s_waitcnt lgkmcnt(8)\n"
    "s_barrier\n"
    "ds_read_b128 a[32:35], v234 offset:192\n"
    "ds_read_b128 a[44:47], v235 offset:192\n"
    "ds_read_b128 a[124:127], v236 offset:192\n"
    "ds_read_b128 a[136:139], v237 offset:192\n"
    "ds_read_b64_tr_b16 a[140:141], v228 offset:6144\n"
    "ds_read_b64_tr_b16 a[142:143], v229 offset:6144\n"
    "ds_read_b64_tr_b16 a[144:145], v230 offset:6144\n"
    "ds_read_b64_tr_b16 a[146:147], v231 offset:6144\n"
    "v_accvgpr_read_b32 v96, a76\n"
    "v_accvgpr_read_b32 v97, a77\n"
    "v_accvgpr_read_b32 v98, a78\n"
    "v_accvgpr_read_b32 v99, a79\n"
    "v_accvgpr_read_b32 v100, a80\n"
    "v_accvgpr_read_b32 v101, a81\n"
    "v_accvgpr_read_b32 v102, a82\n"
    "v_accvgpr_read_b32 v103, a83\n"
    "v_accvgpr_read_b32 v104, a84\n"
    "v_accvgpr_read_b32 v105, a85\n"
    "v_accvgpr_read_b32 v106, a86\n"
    "v_accvgpr_read_b32 v107, a87\n"
    "v_accvgpr_read_b32 v108, a88\n"
    "v_accvgpr_read_b32 v109, a89\n"
    "v_accvgpr_read_b32 v110, a90\n"
    "v_accvgpr_read_b32 v111, a91\n"
    "s_waitcnt lgkmcnt(7)\n"
    "v_mfma_f32_16x16x32_bf16 v[96:99], a[32:35], a[36:39], v[96:99]\n"
    "v_mfma_f32_16x16x32_bf16 v[100:103], a[32:35], a[40:43], v[100:103]\n"
    "s_waitcnt lgkmcnt(6)\n"
    "v_mfma_f32_16x16x32_bf16 v[96:99], a[44:47], a[48:51], v[96:99]\n"
    "v_mfma_f32_16x16x32_bf16 v[100:103], a[44:47], a[52:55], v[100:103]\n"
    "s_waitcnt lgkmcnt(5)\n"
    "v_mfma_f32_16x16x32_bf16 v[104:107], a[124:127], a[128:131], v[104:107]\n"
    "v_mfma_f32_16x16x32_bf16 v[108:111], a[124:127], a[132:135], v[108:111]\n"
    "s_waitcnt lgkmcnt(2)\n"
    "v_mfma_f32_16x16x32_bf16 v[104:107], a[136:139], a[140:143], v[104:107]\n"
    "s_waitcnt lgkmcnt(0)\n"
    "v_mfma_f32_16x16x32_bf16 v[108:111], a[136:139], a[144:147], v[108:111]\n"
    "v_pk_add_f32 v[96:97], v[96:97], v[98:99]\n"
    "v_pk_add_f32 v[100:101], v[100:101], v[102:103]\n"
    "s_nop 3\n"
    "v_pk_add_f32 v[104:105], v[104:105], v[106:107]\n"
    "s_nop 0\n"
    "v_pk_add_f32 v[108:109], v[108:109], v[110:111]\n"
    "v_add_f32 v96, v96, v97\n"
    "v_add_f32 v100, v100, v101\n"
    "v_add_f32 v104, v104, v105\n"
    "v_add_f32 v108, v108, v109\n"
    "v_permlane16_swap_b32 v96, v100\n"
    "s_nop 0\n"
    "v_permlane16_swap_b32 v104, v108\n"
    "v_add_f32 v96, v96, v100\n"
    "v_add_f32 v104, v104, v108\n"
    "v_mul_f32 v96, s64, v96\n"
    "v_mul_f32 v104, s64, v104\n"
    "v_cvt_pk_bf16_f32 v96, v96, v104\n"
    "buffer_store_short v96, v240, s[40:43], 0 offen\n"
    "buffer_store_short_d16_hi v96, v240, s[40:43], 0 offen offset:512\n"
    "s_endpgm\n"
    "L_cpfend_%=:\n"
    "L_pf_%=:\n"
    "s_getpc_b64 s[58:59]\n"
    "L_pc2_%=:\n"
    "s_lshl_b32 s46, s44, 13\n"
    "v_lshl_add_u32 v201, v254, 7, s46\n"
    "v_min_u32 v201, L_end_%=-L_pc2_%=, v201\n"
    "global_load_dword a160, v201, s[58:59]\n"
    "s_branch L_nopf_%=\n"
    "L_ext_%=:\n"
    "v_bfe_u32 v253, v254, 2, 3\n"
    "s_lshl_b32 s47, s44, 6\n"
    "v_lshl_add_u32 v253, v253, 3, s47\n"
    "v_add_u32 v255, 0x14000, v253\n"
    "ds_read_b32 v249, v253 offset:7684\n"
    "ds_read_b32 v250, v253 offset:48644\n"
    "ds_read_b32 v251, v255 offset:7684\n"
    "ds_read_b32 v252, v255 offset:48644\n"
    "s_waitcnt lgkmcnt(0)\n"
    "v_mfma_f32_32x32x16_bf16 v[160:175], v[0:3], a[32:35], v[96:111]\n"
    "s_waitcnt vmcnt(8)\n"
    "s_barrier\n"
    "ds_read_b128 a[32:35], v226 offset:20480\n"
    "v_add_f32 v224, v220, v221\n"
    "v_rcp_f32 v216, v216\n"
    "v_mfma_f32_32x32x16_bf16 v[160:175], v[4:7], a[36:39], v[160:175]\n"
    "ds_read_b128 a[36:39], v227 offset:20480\n"
    "ds_read_b128 a[64:67], v234 offset:64\n"
    "v_cvt_pk_bf16_f32 v224, v224, v224\n"
    "v_rcp_f32 v217, v217\n"
    "v_rcp_f32 v218, v218\n"
    "v_mfma_f32_32x32x16_bf16 v[160:175], v[8:11], a[40:43], v[160:175]\n"
    "ds_read_b128 a[40:43], v226 offset:20992\n"
    "ds_read_b64_tr_b16 a[68:69], v228 offset:10240\n"
    "v_rcp_f32 v219, v219\n"
    "v_exp_f32_e64 v176, v176 clamp\n"
    "v_mfma_f32_32x32x16_bf16 v[160:175], v[12:15], a[44:47], v[160:175]\n"
    "ds_read_b128 a[44:47], v227 offset:20992\n"
    "ds_read_b64_tr_b16 a[70:71], v229 offset:10240\n"
    "v_exp_f32_e64 v177, v177 clamp\n"
    "v_exp_f32_e64 v178, v178 clamp\n"
    "v_mfma_f32_32x32x16_bf16 v[160:175], v[16:19], a[48:51], v[160:175]\n"
    "ds_read_b128 a[48:51], v226 offset:21504\n"
    "ds_read_b64_tr_b16 a[72:73], v230 offset:10240\n"
    "v_perm_b32 v246, v244, v241, s62\n"
    "v_exp_f32_e64 v179, v179 clamp\n"
    "v_exp_f32_e64 v180, v180 clamp\n"
    "v_mfma_f32_32x32x16_bf16 v[160:175], v[20:23], a[52:55], v[160:175]\n"
    "ds_read_b128 a[52:55], v227 offset:21504\n"
    "ds_read_b64_tr_b16 a[74:75], v231 offset:10240\n"
    "v_exp_f32_e64 v181, v181 clamp\n"
    "v_exp_f32_e64 v182, v182 clamp\n"
    "v_mfma_f32_32x32x16_bf16 v[160:175], v[24:27], a[56:59], v[160:175]\n"
    "ds_read_b128 a[56:59], v226 offset:22016\n"
    "ds_write_b16 v232, v224 offset:41024\n"
    "v_exp_f32_e64 v183, v183 clamp\n"
    "v_exp_f32_e64 v184, v184 clamp\n"
    "v_mfma_f32_32x32x16_bf16 v[160:175], v[28:31], a[60:63], v[160:175]\n"
    "s_waitcnt lgkmcnt(2)\n"
    "ds_read_b128 a[60:63], v227 offset:22016\n"
    "v_exp_f32_e64 v185, v185 clamp\n"
    "v_exp_f32_e64 v186, v186 clamp\n"
    "v_exp_f32_e64 v187, v187 clamp\n"
    "v_mfma_f32_16x16x32_bf16 a[76:79], a[64:67], a[68:71], a[76:79]\n"
    "buffer_load_dwordx4 v246, s[92:95], s50 offen offset:2048 nt lds\n"
    "buffer_load_dwordx4 v246, s[92:95], s51 offen offset:3072 nt lds\n"
    "v_exp_f32_e64 v188, v188 clamp\n"
    "v_exp_f32_e64 v189, v189 clamp\n"
    "v_mfma_f32_16x16x32_bf16 a[80:83], a[64:67], a[72:75], a[80:83]\n"
    "v_exp_f32_e64 v190, v190 clamp\n"
    "v_exp_f32_e64 v191, v191 clamp\n"
    "v_pk_fma_f32 v[220:221], v[208:209], v[216:217], v[222:223]\n"
    "v_pk_add_f32 v[176:177], v[176:177], v[192:193]\n"
    "v_pk_add_f32 v[178:179], v[178:179], v[194:195]\n"
    "v_pk_add_f32 v[180:181], v[180:181], v[196:197]\n"
    "v_pk_add_f32 v[182:183], v[182:183], v[198:199]\n"
    "v_pk_fma_f32 v[220:221], v[212:213], v[218:219], v[220:221]\n"
    "v_pk_add_f32 v[184:185], v[184:185], v[200:201]\n"
    "v_pk_add_f32 v[186:187], v[186:187], v[202:203]\n"
    "v_pk_add_f32 v[188:189], v[188:189], v[204:205]\n"
    "v_pk_add_f32 v[190:191], v[190:191], v[206:207]\n"
    "v_pk_add_f32 v[208:209], v[176:177], v[178:179]\n"
    "v_pk_add_f32 v[210:211], v[180:181], v[182:183]\n"
    "v_pk_add_f32 v[212:213], v[184:185], v[186:187]\n"
    "v_pk_add_f32 v[214:215], v[188:189], v[190:191]\n"
    "v_pk_mul_f32 v[176:177], v[176:177], v[178:179]\n"
    "v_pk_mul_f32 v[180:181], v[180:181], v[182:183]\n"
    "v_pk_mul_f32 v[184:185], v[184:185], v[186:187]\n"
    "v_pk_mul_f32 v[188:189], v[188:189], v[190:191]\n"
    "v_pk_mul_f32 v[208:209], v[208:209], v[180:181]\n"
    "v_pk_mul_f32 v[212:213], v[212:213], v[188:189]\n"
    "v_pk_mul_f32 v[216:217], v[176:177], v[180:181]\n"
    "v_pk_mul_f32 v[218:219], v[184:185], v[188:189]\n"
    "v_pk_fma_f32 v[208:209], v[210:211], v[176:177], v[208:209]\n"
    "v_pk_fma_f32 v[212:213], v[214:215], v[184:185], v[212:213]\n"
    "v_mfma_f32_32x32x16_bf16 v[176:191], v[32:35], a[32:35], v[112:127]\n"
    "s_waitcnt vmcnt(8) lgkmcnt(0)\n"
    "s_barrier\n"
    "ds_read_b128 a[32:35], v226 offset:22528\n"
    "v_add_f32 v224, v220, v221\n"
    "v_rcp_f32 v216, v216\n"
    "v_mfma_f32_32x32x16_bf16 v[176:191], v[36:39], a[36:39], v[176:191]\n"
    "ds_read_b128 a[36:39], v227 offset:22528\n"
    "ds_read_b128 a[64:67], v235 offset:64\n"
    "v_cvt_pk_bf16_f32 v224, v224, v224\n"
    "v_rcp_f32 v217, v217\n"
    "v_rcp_f32 v218, v218\n"
    "v_mfma_f32_32x32x16_bf16 v[176:191], v[40:43], a[40:43], v[176:191]\n"
    "ds_read_b128 a[40:43], v226 offset:23040\n"
    "ds_read_b64_tr_b16 a[68:69], v228 offset:12288\n"
    "v_rcp_f32 v219, v219\n"
    "v_exp_f32_e64 v160, v160 clamp\n"
    "v_mfma_f32_32x32x16_bf16 v[176:191], v[44:47], a[44:47], v[176:191]\n"
    "ds_read_b128 a[44:47], v227 offset:23040\n"
    "ds_read_b64_tr_b16 a[70:71], v229 offset:12288\n"
    "v_exp_f32_e64 v161, v161 clamp\n"
    "v_exp_f32_e64 v162, v162 clamp\n"
    "v_mfma_f32_32x32x16_bf16 v[176:191], v[48:51], a[48:51], v[176:191]\n"
    "ds_read_b128 a[48:51], v226 offset:23552\n"
    "ds_read_b64_tr_b16 a[72:73], v230 offset:12288\n"
    "v_perm_b32 v247, v245, v241, s62\n"
    "v_exp_f32_e64 v163, v163 clamp\n"
    "v_exp_f32_e64 v164, v164 clamp\n"
    "v_mfma_f32_32x32x16_bf16 v[176:191], v[52:55], a[52:55], v[176:191]\n"
    "s_add_u32 m0, s80, 0x1000\n"
    "ds_read_b128 a[52:55], v227 offset:23552\n"
    "ds_read_b64_tr_b16 a[74:75], v231 offset:12288\n"
    "v_exp_f32_e64 v165, v165 clamp\n"
    "v_exp_f32_e64 v166, v166 clamp\n"
    "v_mfma_f32_32x32x16_bf16 v[176:191], v[56:59], a[56:59], v[176:191]\n"
    "ds_read_b128 a[56:59], v226 offset:24064\n"
    "ds_write_b16 v233, v224 offset:64\n"
    "v_exp_f32_e64 v167, v167 clamp\n"
    "v_exp_f32_e64 v168, v168 clamp\n"
    "v_mfma_f32_32x32x16_bf16 v[176:191], v[60:63], a[60:63], v[176:191]\n"
    "s_waitcnt lgkmcnt(2)\n"
    "ds_read_b128 a[60:63], v227 offset:24064\n"
    "v_exp_f32_e64 v169, v169 clamp\n"
    "v_exp_f32_e64 v170, v170 clamp\n"
    "v_exp_f32_e64 v171, v171 clamp\n"
    "v_mfma_f32_16x16x32_bf16 a[76:79], a[64:67], a[68:71], a[76:79]\n"
    "buffer_load_dwordx4 v247, s[96:99], s50 offen offset:2048 nt lds\n"
    "buffer_load_dwordx4 v247, s[96:99], s51 offen offset:3072 nt lds\n"
    "v_exp_f32_e64 v172, v172 clamp\n"
    "v_exp_f32_e64 v173, v173 clamp\n"
    "v_mfma_f32_16x16x32_bf16 a[80:83], a[64:67], a[72:75], a[80:83]\n"
    "v_exp_f32_e64 v174, v174 clamp\n"
    "v_exp_f32_e64 v175, v175 clamp\n"
    "v_pk_fma_f32 v[220:221], v[208:209], v[216:217], v[222:223]\n"
    "v_pk_add_f32 v[160:161], v[160:161], v[192:193]\n"
    "v_pk_add_f32 v[162:163], v[162:163], v[194:195]\n"
    "v_pk_add_f32 v[164:165], v[164:165], v[196:197]\n"
    "v_pk_add_f32 v[166:167], v[166:167], v[198:199]\n"
    "v_pk_fma_f32 v[220:221], v[212:213], v[218:219], v[220:221]\n"
    "v_pk_add_f32 v[168:169], v[168:169], v[200:201]\n"
    "v_pk_add_f32 v[170:171], v[170:171], v[202:203]\n"
    "v_pk_add_f32 v[172:173], v[172:173], v[204:205]\n"
    "v_pk_add_f32 v[174:175], v[174:175], v[206:207]\n"
    "v_pk_add_f32 v[208:209], v[160:161], v[162:163]\n"
    "v_pk_add_f32 v[210:211], v[164:165], v[166:167]\n"
    "v_pk_add_f32 v[212:213], v[168:169], v[170:171]\n"
    "v_pk_add_f32 v[214:215], v[172:173], v[174:175]\n"
    "v_pk_mul_f32 v[160:161], v[160:161], v[162:163]\n"
    "v_pk_mul_f32 v[164:165], v[164:165], v[166:167]\n"
    "v_pk_mul_f32 v[168:169], v[168:169], v[170:171]\n"
    "v_pk_mul_f32 v[172:173], v[172:173], v[174:175]\n"
    "v_pk_mul_f32 v[208:209], v[208:209], v[164:165]\n"
    "v_pk_mul_f32 v[212:213], v[212:213], v[172:173]\n"
    "v_pk_mul_f32 v[216:217], v[160:161], v[164:165]\n"
    "v_pk_mul_f32 v[218:219], v[168:169], v[172:173]\n"
    "v_pk_fma_f32 v[208:209], v[210:211], v[160:161], v[208:209]\n"
    "v_pk_fma_f32 v[212:213], v[214:215], v[168:169], v[212:213]\n"
    "v_mfma_f32_32x32x16_bf16 v[160:175], v[64:67], a[32:35], v[128:143]\n"
    "s_waitcnt vmcnt(8) lgkmcnt(0)\n"
    "s_barrier\n"
    "ds_read_b128 a[32:35], v226 offset:24576\n"
    "v_add_f32 v224, v220, v221\n"
    "v_rcp_f32 v216, v216\n"
    "v_mfma_f32_32x32x16_bf16 v[160:175], v[68:71], a[36:39], v[160:175]\n"
    "ds_read_b128 a[36:39], v227 offset:24576\n"
    "ds_read_b128 a[64:67], v236 offset:64\n"
    "v_cvt_pk_bf16_f32 v224, v224, v224\n"
    "v_rcp_f32 v217, v217\n"
    "v_rcp_f32 v218, v218\n"
    "v_mfma_f32_32x32x16_bf16 v[160:175], v[72:75], a[40:43], v[160:175]\n"
    "ds_read_b128 a[40:43], v226 offset:25088\n"
    "ds_read_b64_tr_b16 a[68:69], v228 offset:14336\n"
    "v_rcp_f32 v219, v219\n"
    "v_exp_f32_e64 v176, v176 clamp\n"
    "v_mfma_f32_32x32x16_bf16 v[160:175], v[76:79], a[44:47], v[160:175]\n"
    "ds_read_b128 a[44:47], v227 offset:25088\n"
    "ds_read_b64_tr_b16 a[70:71], v229 offset:14336\n"
    "v_exp_f32_e64 v177, v177 clamp\n"
    "v_exp_f32_e64 v178, v178 clamp\n"
    "v_mfma_f32_32x32x16_bf16 v[160:175], v[80:83], a[48:51], v[160:175]\n"
    "ds_read_b128 a[48:51], v226 offset:25600\n"
    "ds_read_b64_tr_b16 a[72:73], v230 offset:14336\n"
    "v_perm_b32 v246, v249, v241, s60\n"
    "v_exp_f32_e64 v179, v179 clamp\n"
    "v_exp_f32_e64 v180, v180 clamp\n"
    "v_mfma_f32_32x32x16_bf16 v[160:175], v[84:87], a[52:55], v[160:175]\n"
    "s_add_u32 m0, s80, 0x0\n"
    "ds_read_b128 a[52:55], v227 offset:25600\n"
    "ds_read_b64_tr_b16 a[74:75], v231 offset:14336\n"
    "v_exp_f32_e64 v181, v181 clamp\n"
    "v_exp_f32_e64 v182, v182 clamp\n"
    "v_mfma_f32_32x32x16_bf16 v[160:175], v[88:91], a[56:59], v[160:175]\n"
    "ds_read_b128 a[56:59], v226 offset:26112\n"
    "ds_write_b16 v233, v224 offset:41024\n"
    "v_exp_f32_e64 v183, v183 clamp\n"
    "v_exp_f32_e64 v184, v184 clamp\n"
    "v_mfma_f32_32x32x16_bf16 v[160:175], v[92:95], a[60:63], v[160:175]\n"
    "s_waitcnt lgkmcnt(2)\n"
    "ds_read_b128 a[60:63], v227 offset:26112\n"
    "v_exp_f32_e64 v185, v185 clamp\n"
    "v_exp_f32_e64 v186, v186 clamp\n"
    "v_exp_f32_e64 v187, v187 clamp\n"
    "v_mfma_f32_16x16x32_bf16 a[84:87], a[64:67], a[68:71], a[84:87]\n"
    "buffer_load_dwordx4 v246, s[84:87], s48 offen offset:0 nt lds\n"
    "buffer_load_dwordx4 v246, s[84:87], s49 offen offset:1024 nt lds\n"
    "v_exp_f32_e64 v188, v188 clamp\n"
    "v_exp_f32_e64 v189, v189 clamp\n"
    "v_mfma_f32_16x16x32_bf16 a[88:91], a[64:67], a[72:75], a[88:91]\n"
    "v_exp_f32_e64 v190, v190 clamp\n"
    "v_exp_f32_e64 v191, v191 clamp\n"
    "v_pk_fma_f32 v[220:221], v[208:209], v[216:217], v[222:223]\n"
    "v_pk_add_f32 v[176:177], v[176:177], v[192:193]\n"
    "v_pk_add_f32 v[178:179], v[178:179], v[194:195]\n"
    "v_pk_add_f32 v[180:181], v[180:181], v[196:197]\n"
    "v_pk_add_f32 v[182:183], v[182:183], v[198:199]\n"
    "v_pk_fma_f32 v[220:221], v[212:213], v[218:219], v[220:221]\n"
    "v_pk_add_f32 v[184:185], v[184:185], v[200:201]\n"
    "v_pk_add_f32 v[186:187], v[186:187], v[202:203]\n"
    "v_pk_add_f32 v[188:189], v[188:189], v[204:205]\n"
    "v_pk_add_f32 v[190:191], v[190:191], v[206:207]\n"
    "v_pk_add_f32 v[208:209], v[176:177], v[178:179]\n"
    "v_pk_add_f32 v[210:211], v[180:181], v[182:183]\n"
    "v_pk_add_f32 v[212:213], v[184:185], v[186:187]\n"
    "v_pk_add_f32 v[214:215], v[188:189], v[190:191]\n"
    "v_pk_mul_f32 v[176:177], v[176:177], v[178:179]\n"
    "v_pk_mul_f32 v[180:181], v[180:181], v[182:183]\n"
    "v_pk_mul_f32 v[184:185], v[184:185], v[186:187]\n"
    "v_pk_mul_f32 v[188:189], v[188:189], v[190:191]\n"
    "v_pk_mul_f32 v[208:209], v[208:209], v[180:181]\n"
    "v_pk_mul_f32 v[212:213], v[212:213], v[188:189]\n"
    "v_pk_mul_f32 v[216:217], v[176:177], v[180:181]\n"
    "v_pk_mul_f32 v[218:219], v[184:185], v[188:189]\n"
    "v_pk_fma_f32 v[208:209], v[210:211], v[176:177], v[208:209]\n"
    "v_pk_fma_f32 v[212:213], v[214:215], v[184:185], v[212:213]\n"
    "v_mfma_f32_32x32x16_bf16 v[176:191], a[0:3], a[32:35], v[144:159]\n"
    "s_waitcnt vmcnt(8) lgkmcnt(0)\n"
    "s_barrier\n"
    "ds_read_b128 a[32:35], v226 offset:26624\n"
    "v_add_f32 v224, v220, v221\n"
    "v_rcp_f32 v216, v216\n"
    "v_mfma_f32_32x32x16_bf16 v[176:191], a[4:7], a[36:39], v[176:191]\n"
    "ds_read_b128 a[36:39], v227 offset:26624\n"
    "ds_read_b128 a[64:67], v237 offset:64\n"
    "v_cvt_pk_bf16_f32 v224, v224, v224\n"
    "v_rcp_f32 v217, v217\n"
    "v_rcp_f32 v218, v218\n"
    "v_mfma_f32_32x32x16_bf16 v[176:191], a[8:11], a[40:43], v[176:191]\n"
    "ds_read_b128 a[40:43], v226 offset:27136\n"
    "ds_read_b64_tr_b16 a[68:69], v228 offset:16384\n"
    "v_rcp_f32 v219, v219\n"
    "v_exp_f32_e64 v160, v160 clamp\n"
    "v_mfma_f32_32x32x16_bf16 v[176:191], a[12:15], a[44:47], v[176:191]\n"
    "ds_read_b128 a[44:47], v227 offset:27136\n"
    "ds_read_b64_tr_b16 a[70:71], v229 offset:16384\n"
    "v_exp_f32_e64 v161, v161 clamp\n"
    "v_exp_f32_e64 v162, v162 clamp\n"
    "v_mfma_f32_32x32x16_bf16 v[176:191], a[16:19], a[48:51], v[176:191]\n"
    "ds_read_b128 a[48:51], v226 offset:27648\n"
    "ds_read_b64_tr_b16 a[72:73], v230 offset:16384\n"
    "v_perm_b32 v247, v250, v241, s60\n"
    "v_exp_f32_e64 v163, v163 clamp\n"
    "v_exp_f32_e64 v164, v164 clamp\n"
    "v_mfma_f32_32x32x16_bf16 v[176:191], a[20:23], a[52:55], v[176:191]\n"
    "ds_read_b128 a[52:55], v227 offset:27648\n"
    "ds_read_b64_tr_b16 a[74:75], v231 offset:16384\n"
    "v_exp_f32_e64 v165, v165 clamp\n"
    "v_exp_f32_e64 v166, v166 clamp\n"
    "v_mfma_f32_32x32x16_bf16 v[176:191], a[24:27], a[56:59], v[176:191]\n"
    "ds_read_b128 a[56:59], v226 offset:28160\n"
    "ds_write_b16 v232, v224 offset:128\n"
    "v_exp_f32_e64 v167, v167 clamp\n"
    "v_exp_f32_e64 v168, v168 clamp\n"
    "v_mfma_f32_32x32x16_bf16 v[176:191], a[28:31], a[60:63], v[176:191]\n"
    "s_waitcnt lgkmcnt(2)\n"
    "ds_read_b128 a[60:63], v227 offset:28160\n"
    "v_exp_f32_e64 v169, v169 clamp\n"
    "v_exp_f32_e64 v170, v170 clamp\n"
    "v_exp_f32_e64 v171, v171 clamp\n"
    "v_mfma_f32_16x16x32_bf16 a[84:87], a[64:67], a[68:71], a[84:87]\n"
    "buffer_load_dwordx4 v247, s[88:91], s50 offen offset:2048 nt lds\n"
    "buffer_load_dwordx4 v247, s[88:91], s51 offen offset:3072 nt lds\n"
    "v_exp_f32_e64 v172, v172 clamp\n"
    "v_exp_f32_e64 v173, v173 clamp\n"
    "v_mfma_f32_16x16x32_bf16 a[88:91], a[64:67], a[72:75], a[88:91]\n"
    "v_exp_f32_e64 v174, v174 clamp\n"
    "v_exp_f32_e64 v175, v175 clamp\n"
    "v_pk_fma_f32 v[220:221], v[208:209], v[216:217], v[222:223]\n"
    "v_pk_add_f32 v[160:161], v[160:161], v[192:193]\n"
    "v_pk_add_f32 v[162:163], v[162:163], v[194:195]\n"
    "v_pk_add_f32 v[164:165], v[164:165], v[196:197]\n"
    "v_pk_add_f32 v[166:167], v[166:167], v[198:199]\n"
    "v_pk_fma_f32 v[220:221], v[212:213], v[218:219], v[220:221]\n"
    "v_pk_add_f32 v[168:169], v[168:169], v[200:201]\n"
    "v_pk_add_f32 v[170:171], v[170:171], v[202:203]\n"
    "v_pk_add_f32 v[172:173], v[172:173], v[204:205]\n"
    "v_pk_add_f32 v[174:175], v[174:175], v[206:207]\n"
    "v_pk_add_f32 v[208:209], v[160:161], v[162:163]\n"
    "v_pk_add_f32 v[210:211], v[164:165], v[166:167]\n"
    "v_pk_add_f32 v[212:213], v[168:169], v[170:171]\n"
    "v_pk_add_f32 v[214:215], v[172:173], v[174:175]\n"
    "v_pk_mul_f32 v[160:161], v[160:161], v[162:163]\n"
    "v_pk_mul_f32 v[164:165], v[164:165], v[166:167]\n"
    "v_pk_mul_f32 v[168:169], v[168:169], v[170:171]\n"
    "v_pk_mul_f32 v[172:173], v[172:173], v[174:175]\n"
    "v_pk_mul_f32 v[208:209], v[208:209], v[164:165]\n"
    "v_pk_mul_f32 v[212:213], v[212:213], v[172:173]\n"
    "v_pk_mul_f32 v[216:217], v[160:161], v[164:165]\n"
    "v_pk_mul_f32 v[218:219], v[168:169], v[172:173]\n"
    "v_pk_fma_f32 v[208:209], v[210:211], v[160:161], v[208:209]\n"
    "v_pk_fma_f32 v[212:213], v[214:215], v[168:169], v[212:213]\n"
    "v_mfma_f32_32x32x16_bf16 v[160:175], v[0:3], a[32:35], v[96:111]\n"
    "s_waitcnt vmcnt(8) lgkmcnt(0)\n"
    "s_barrier\n"
    "ds_read_b128 a[32:35], v226 offset:28672\n"
    "v_add_f32 v224, v220, v221\n"
    "v_rcp_f32 v216, v216\n"
    "v_mfma_f32_32x32x16_bf16 v[160:175], v[4:7], a[36:39], v[160:175]\n"
    "ds_read_b128 a[36:39], v227 offset:28672\n"
    "ds_read_b128 a[64:67], v234 offset:128\n"
    "v_cvt_pk_bf16_f32 v224, v224, v224\n"
    "v_rcp_f32 v217, v217\n"
    "v_rcp_f32 v218, v218\n"
    "v_mfma_f32_32x32x16_bf16 v[160:175], v[8:11], a[40:43], v[160:175]\n"
    "ds_read_b128 a[40:43], v226 offset:29184\n"
    "ds_read_b64_tr_b16 a[68:69], v228 offset:18432\n"
    "v_rcp_f32 v219, v219\n"
    "v_exp_f32_e64 v176, v176 clamp\n"
    "v_mfma_f32_32x32x16_bf16 v[160:175], v[12:15], a[44:47], v[160:175]\n"
    "ds_read_b128 a[44:47], v227 offset:29184\n"
    "ds_read_b64_tr_b16 a[70:71], v229 offset:18432\n"
    "v_exp_f32_e64 v177, v177 clamp\n"
    "v_exp_f32_e64 v178, v178 clamp\n"
    "v_mfma_f32_32x32x16_bf16 v[160:175], v[16:19], a[48:51], v[160:175]\n"
    "ds_read_b128 a[48:51], v226 offset:29696\n"
    "ds_read_b64_tr_b16 a[72:73], v230 offset:18432\n"
    "v_perm_b32 v246, v251, v241, s60\n"
    "v_exp_f32_e64 v179, v179 clamp\n"
    "v_exp_f32_e64 v180, v180 clamp\n"
    "v_mfma_f32_32x32x16_bf16 v[160:175], v[20:23], a[52:55], v[160:175]\n"
    "s_add_u32 m0, s80, 0x1000\n"
    "ds_read_b128 a[52:55], v227 offset:29696\n"
    "ds_read_b64_tr_b16 a[74:75], v231 offset:18432\n"
    "v_exp_f32_e64 v181, v181 clamp\n"
    "v_exp_f32_e64 v182, v182 clamp\n"
    "v_mfma_f32_32x32x16_bf16 v[160:175], v[24:27], a[56:59], v[160:175]\n"
    "ds_read_b128 a[56:59], v226 offset:30208\n"
    "ds_write_b16 v232, v224 offset:41088\n"
    "v_exp_f32_e64 v183, v183 clamp\n"
    "v_exp_f32_e64 v184, v184 clamp\n"
    "v_mfma_f32_32x32x16_bf16 v[160:175], v[28:31], a[60:63], v[160:175]\n"
    "s_waitcnt lgkmcnt(2)\n"
    "ds_read_b128 a[60:63], v227 offset:30208\n"
    "v_exp_f32_e64 v185, v185 clamp\n"
    "v_exp_f32_e64 v186, v186 clamp\n"
    "v_exp_f32_e64 v187, v187 clamp\n"
    "v_mfma_f32_16x16x32_bf16 a[76:79], a[64:67], a[68:71], a[76:79]\n"
    "buffer_load_dwordx4 v246, s[92:95], s48 offen offset:0 nt lds\n"
    "buffer_load_dwordx4 v246, s[92:95], s49 offen offset:1024 nt lds\n"
    "v_exp_f32_e64 v188, v188 clamp\n"
    "v_exp_f32_e64 v189, v189 clamp\n"
    "v_mfma_f32_16x16x32_bf16 a[80:83], a[64:67], a[72:75], a[80:83]\n"
    "v_exp_f32_e64 v190, v190 clamp\n"
    "v_exp_f32_e64 v191, v191 clamp\n"
    "v_pk_fma_f32 v[220:221], v[208:209], v[216:217], v[222:223]\n"
    "v_pk_add_f32 v[176:177], v[176:177], v[192:193]\n"
    "v_pk_add_f32 v[178:179], v[178:179], v[194:195]\n"
    "v_pk_add_f32 v[180:181], v[180:181], v[196:197]\n"
    "v_pk_add_f32 v[182:183], v[182:183], v[198:199]\n"
    "v_pk_fma_f32 v[220:221], v[212:213], v[218:219], v[220:221]\n"
    "v_pk_add_f32 v[184:185], v[184:185], v[200:201]\n"
    "v_pk_add_f32 v[186:187], v[186:187], v[202:203]\n"
    "v_pk_add_f32 v[188:189], v[188:189], v[204:205]\n"
    "v_pk_add_f32 v[190:191], v[190:191], v[206:207]\n"
    "v_pk_add_f32 v[208:209], v[176:177], v[178:179]\n"
    "v_pk_add_f32 v[210:211], v[180:181], v[182:183]\n"
    "v_pk_add_f32 v[212:213], v[184:185], v[186:187]\n"
    "v_pk_add_f32 v[214:215], v[188:189], v[190:191]\n"
    "v_pk_mul_f32 v[176:177], v[176:177], v[178:179]\n"
    "v_pk_mul_f32 v[180:181], v[180:181], v[182:183]\n"
    "v_pk_mul_f32 v[184:185], v[184:185], v[186:187]\n"
    "v_pk_mul_f32 v[188:189], v[188:189], v[190:191]\n"
    "v_pk_mul_f32 v[208:209], v[208:209], v[180:181]\n"
    "v_pk_mul_f32 v[212:213], v[212:213], v[188:189]\n"
    "v_pk_mul_f32 v[216:217], v[176:177], v[180:181]\n"
    "v_pk_mul_f32 v[218:219], v[184:185], v[188:189]\n"
    "v_pk_fma_f32 v[208:209], v[210:211], v[176:177], v[208:209]\n"
    "v_pk_fma_f32 v[212:213], v[214:215], v[184:185], v[212:213]\n"
    "v_mfma_f32_32x32x16_bf16 v[176:191], v[32:35], a[32:35], v[112:127]\n"
    "s_waitcnt vmcnt(8) lgkmcnt(0)\n"
    "s_barrier\n"
    "ds_read_b128 a[32:35], v226 offset:30720\n"
    "v_add_f32 v224, v220, v221\n"
    "v_rcp_f32 v216, v216\n"
    "v_mfma_f32_32x32x16_bf16 v[176:191], v[36:39], a[36:39], v[176:191]\n"
    "ds_read_b128 a[36:39], v227 offset:30720\n"
    "ds_read_b128 a[64:67], v235 offset:128\n"
    "v_cvt_pk_bf16_f32 v224, v224, v224\n"
    "v_rcp_f32 v217, v217\n"
    "v_rcp_f32 v218, v218\n"
    "v_mfma_f32_32x32x16_bf16 v[176:191], v[40:43], a[40:43], v[176:191]\n"
    "ds_read_b128 a[40:43], v226 offset:31232\n"
    "ds_read_b64_tr_b16 a[68:69], v228 offset:20480\n"
    "v_rcp_f32 v219, v219\n"
    "v_exp_f32_e64 v160, v160 clamp\n"
    "v_mfma_f32_32x32x16_bf16 v[176:191], v[44:47], a[44:47], v[176:191]\n"
    "ds_read_b128 a[44:47], v227 offset:31232\n"
    "ds_read_b64_tr_b16 a[70:71], v229 offset:20480\n"
    "v_exp_f32_e64 v161, v161 clamp\n"
    "v_exp_f32_e64 v162, v162 clamp\n"
    "v_mfma_f32_32x32x16_bf16 v[176:191], v[48:51], a[48:51], v[176:191]\n"
    "ds_read_b128 a[48:51], v226 offset:31744\n"
    "ds_read_b64_tr_b16 a[72:73], v230 offset:20480\n"
    "v_perm_b32 v247, v252, v241, s60\n"
    "v_exp_f32_e64 v163, v163 clamp\n"
    "v_exp_f32_e64 v164, v164 clamp\n"
    "v_mfma_f32_32x32x16_bf16 v[176:191], v[52:55], a[52:55], v[176:191]\n"
    "s_add_u32 m0, s80, 0x2000\n"
    "ds_read_b128 a[52:55], v227 offset:31744\n"
    "ds_read_b64_tr_b16 a[74:75], v231 offset:20480\n"
    "v_exp_f32_e64 v165, v165 clamp\n"
    "v_exp_f32_e64 v166, v166 clamp\n"
    "v_mfma_f32_32x32x16_bf16 v[176:191], v[56:59], a[56:59], v[176:191]\n"
    "ds_read_b128 a[56:59], v226 offset:32256\n"
    "ds_write_b16 v233, v224 offset:128\n"
    "v_exp_f32_e64 v167, v167 clamp\n"
    "v_exp_f32_e64 v168, v168 clamp\n"
    "v_mfma_f32_32x32x16_bf16 v[176:191], v[60:63], a[60:63], v[176:191]\n"
    "s_waitcnt lgkmcnt(2)\n"
    "ds_read_b128 a[60:63], v227 offset:32256\n"
    "v_exp_f32_e64 v169, v169 clamp\n"
    "v_exp_f32_e64 v170, v170 clamp\n"
    "v_exp_f32_e64 v171, v171 clamp\n"
    "v_mfma_f32_16x16x32_bf16 a[76:79], a[64:67], a[68:71], a[76:79]\n"
    "buffer_load_dwordx4 v247, s[96:99], s48 offen offset:0 nt lds\n"
    "buffer_load_dwordx4 v247, s[96:99], s49 offen offset:1024 nt lds\n"
    "v_exp_f32_e64 v172, v172 clamp\n"
    "v_exp_f32_e64 v173, v173 clamp\n"
    "v_mfma_f32_16x16x32_bf16 a[80:83], a[64:67], a[72:75], a[80:83]\n"
    "v_exp_f32_e64 v174, v174 clamp\n"
    "v_exp_f32_e64 v175, v175 clamp\n"
    "v_pk_fma_f32 v[220:221], v[208:209], v[216:217], v[222:223]\n"
    "v_pk_add_f32 v[160:161], v[160:161], v[192:193]\n"
    "v_pk_add_f32 v[162:163], v[162:163], v[194:195]\n"
    "v_pk_add_f32 v[164:165], v[164:165], v[196:197]\n"
    "v_pk_add_f32 v[166:167], v[166:167], v[198:199]\n"
    "v_pk_fma_f32 v[220:221], v[212:213], v[218:219], v[220:221]\n"
    "v_pk_add_f32 v[168:169], v[168:169], v[200:201]\n"
    "v_pk_add_f32 v[170:171], v[170:171], v[202:203]\n"
    "v_pk_add_f32 v[172:173], v[172:173], v[204:205]\n"
    "v_pk_add_f32 v[174:175], v[174:175], v[206:207]\n"
    "v_pk_add_f32 v[208:209], v[160:161], v[162:163]\n"
    "v_pk_add_f32 v[210:211], v[164:165], v[166:167]\n"
    "v_pk_add_f32 v[212:213], v[168:169], v[170:171]\n"
    "v_pk_add_f32 v[214:215], v[172:173], v[174:175]\n"
    "v_pk_mul_f32 v[160:161], v[160:161], v[162:163]\n"
    "v_pk_mul_f32 v[164:165], v[164:165], v[166:167]\n"
    "v_pk_mul_f32 v[168:169], v[168:169], v[170:171]\n"
    "v_pk_mul_f32 v[172:173], v[172:173], v[174:175]\n"
    "v_pk_mul_f32 v[208:209], v[208:209], v[164:165]\n"
    "v_pk_mul_f32 v[212:213], v[212:213], v[172:173]\n"
    "v_pk_mul_f32 v[216:217], v[160:161], v[164:165]\n"
    "v_pk_mul_f32 v[218:219], v[168:169], v[172:173]\n"
    "v_pk_fma_f32 v[208:209], v[210:211], v[160:161], v[208:209]\n"
    "v_pk_fma_f32 v[212:213], v[214:215], v[168:169], v[212:213]\n"
    "v_mfma_f32_32x32x16_bf16 v[160:175], v[64:67], a[32:35], v[128:143]\n"
    "s_waitcnt vmcnt(8) lgkmcnt(0)\n"
    "s_barrier\n"
    "ds_read_b128 a[32:35], v226 offset:6144\n"
    "v_add_f32 v224, v220, v221\n"
    "v_rcp_f32 v216, v216\n"
    "v_mfma_f32_32x32x16_bf16 v[160:175], v[68:71], a[36:39], v[160:175]\n"
    "ds_read_b128 a[36:39], v227 offset:6144\n"
    "ds_read_b128 a[64:67], v236 offset:128\n"
    "v_cvt_pk_bf16_f32 v224, v224, v224\n"
    "v_rcp_f32 v217, v217\n"
    "v_rcp_f32 v218, v218\n"
    "v_mfma_f32_32x32x16_bf16 v[160:175], v[72:75], a[40:43], v[160:175]\n"
    "ds_read_b128 a[40:43], v226 offset:6656\n"
    "ds_read_b64_tr_b16 a[68:69], v228 offset:22528\n"
    "v_rcp_f32 v219, v219\n"
    "v_exp_f32_e64 v176, v176 clamp\n"
    "v_mfma_f32_32x32x16_bf16 v[160:175], v[76:79], a[44:47], v[160:175]\n"
    "ds_read_b128 a[44:47], v227 offset:6656\n"
    "ds_read_b64_tr_b16 a[70:71], v229 offset:22528\n"
    "v_exp_f32_e64 v177, v177 clamp\n"
    "v_exp_f32_e64 v178, v178 clamp\n"
    "v_mfma_f32_32x32x16_bf16 v[160:175], v[80:83], a[48:51], v[160:175]\n"
    "ds_read_b128 a[48:51], v226 offset:7168\n"
    "ds_read_b64_tr_b16 a[72:73], v230 offset:22528\n"
    "v_exp_f32_e64 v179, v179 clamp\n"
    "v_exp_f32_e64 v180, v180 clamp\n"
    "v_mfma_f32_32x32x16_bf16 v[160:175], v[84:87], a[52:55], v[160:175]\n"
    "ds_read_b128 a[52:55], v227 offset:7168\n"
    "ds_read_b64_tr_b16 a[74:75], v231 offset:22528\n"
    "v_exp_f32_e64 v181, v181 clamp\n"
    "v_exp_f32_e64 v182, v182 clamp\n"
    "v_mfma_f32_32x32x16_bf16 v[160:175], v[88:91], a[56:59], v[160:175]\n"
    "ds_read_b128 a[56:59], v226 offset:7680\n"
    "ds_write_b16 v233, v224 offset:41088\n"
    "v_exp_f32_e64 v183, v183 clamp\n"
    "v_exp_f32_e64 v184, v184 clamp\n"
    "v_mfma_f32_32x32x16_bf16 v[160:175], v[92:95], a[60:63], v[160:175]\n"
    "s_waitcnt lgkmcnt(2)\n"
    "ds_read_b128 a[60:63], v227 offset:7680\n"
    "v_exp_f32_e64 v185, v185 clamp\n"
    "v_exp_f32_e64 v186, v186 clamp\n"
    "v_exp_f32_e64 v187, v187 clamp\n"
    "v_mfma_f32_16x16x32_bf16 a[84:87], a[64:67], a[68:71], a[84:87]\n"
    "v_exp_f32_e64 v188, v188 clamp\n"
    "v_exp_f32_e64 v189, v189 clamp\n"
    "v_mfma_f32_16x16x32_bf16 a[88:91], a[64:67], a[72:75], a[88:91]\n"
    "v_exp_f32_e64 v190, v190 clamp\n"
    "v_exp_f32_e64 v191, v191 clamp\n"
    "v_pk_fma_f32 v[220:221], v[208:209], v[216:217], v[222:223]\n"
    "v_pk_add_f32 v[176:177], v[176:177], v[192:193]\n"
    "v_pk_add_f32 v[178:179], v[178:179], v[194:195]\n"
    "v_pk_add_f32 v[180:181], v[180:181], v[196:197]\n"
    "v_pk_add_f32 v[182:183], v[182:183], v[198:199]\n"
    "v_pk_fma_f32 v[220:221], v[212:213], v[218:219], v[220:221]\n"
    "v_pk_add_f32 v[184:185], v[184:185], v[200:201]\n"
    "v_pk_add_f32 v[186:187], v[186:187], v[202:203]\n"
    "v_pk_add_f32 v[188:189], v[188:189], v[204:205]\n"
    "v_pk_add_f32 v[190:191], v[190:191], v[206:207]\n"
    "v_pk_add_f32 v[208:209], v[176:177], v[178:179]\n"
    "v_pk_add_f32 v[210:211], v[180:181], v[182:183]\n"
    "v_pk_add_f32 v[212:213], v[184:185], v[186:187]\n"
    "v_pk_add_f32 v[214:215], v[188:189], v[190:191]\n"
    "v_pk_mul_f32 v[176:177], v[176:177], v[178:179]\n"
    "v_pk_mul_f32 v[180:181], v[180:181], v[182:183]\n"
    "v_pk_mul_f32 v[184:185], v[184:185], v[186:187]\n"
    "v_pk_mul_f32 v[188:189], v[188:189], v[190:191]\n"
    "v_pk_mul_f32 v[208:209], v[208:209], v[180:181]\n"
    "v_pk_mul_f32 v[212:213], v[212:213], v[188:189]\n"
    "v_pk_mul_f32 v[216:217], v[176:177], v[180:181]\n"
    "v_pk_mul_f32 v[218:219], v[184:185], v[188:189]\n"
    "v_pk_fma_f32 v[208:209], v[210:211], v[176:177], v[208:209]\n"
    "v_pk_fma_f32 v[212:213], v[214:215], v[184:185], v[212:213]\n"
    "v_mfma_f32_32x32x16_bf16 v[176:191], a[0:3], a[32:35], v[144:159]\n"
    "s_waitcnt vmcnt(6) lgkmcnt(0)\n"
    "s_barrier\n"
    "ds_read_b128 a[32:35], v226 offset:0\n"
    "v_add_f32 v224, v220, v221\n"
    "v_rcp_f32 v216, v216\n"
    "v_mfma_f32_32x32x16_bf16 v[176:191], a[4:7], a[36:39], v[176:191]\n"
    "ds_read_b128 a[36:39], v227 offset:0\n"
    "ds_read_b128 a[64:67], v237 offset:128\n"
    "v_cvt_pk_bf16_f32 v224, v224, v224\n"
    "v_rcp_f32 v217, v217\n"
    "v_rcp_f32 v218, v218\n"
    "v_mfma_f32_32x32x16_bf16 v[176:191], a[8:11], a[40:43], v[176:191]\n"
    "ds_read_b128 a[40:43], v226 offset:512\n"
    "ds_read_b64_tr_b16 a[68:69], v228 offset:24576\n"
    "v_rcp_f32 v219, v219\n"
    "v_exp_f32_e64 v160, v160 clamp\n"
    "v_mfma_f32_32x32x16_bf16 v[176:191], a[12:15], a[44:47], v[176:191]\n"
    "ds_read_b128 a[44:47], v227 offset:512\n"
    "ds_read_b64_tr_b16 a[70:71], v229 offset:24576\n"
    "v_exp_f32_e64 v161, v161 clamp\n"
    "v_exp_f32_e64 v162, v162 clamp\n"
    "v_mfma_f32_32x32x16_bf16 v[176:191], a[16:19], a[48:51], v[176:191]\n"
    "ds_read_b128 a[48:51], v226 offset:1024\n"
    "ds_read_b64_tr_b16 a[72:73], v230 offset:24576\n"
    "v_exp_f32_e64 v163, v163 clamp\n"
    "v_exp_f32_e64 v164, v164 clamp\n"
    "v_mfma_f32_32x32x16_bf16 v[176:191], a[20:23], a[52:55], v[176:191]\n"
    "ds_read_b128 a[52:55], v227 offset:1024\n"
    "ds_read_b64_tr_b16 a[74:75], v231 offset:24576\n"
    "v_exp_f32_e64 v165, v165 clamp\n"
    "v_exp_f32_e64 v166, v166 clamp\n"
    "v_mfma_f32_32x32x16_bf16 v[176:191], a[24:27], a[56:59], v[176:191]\n"
    "ds_read_b128 a[56:59], v226 offset:1536\n"
    "ds_write_b16 v232, v224 offset:192\n"
    "v_exp_f32_e64 v167, v167 clamp\n"
    "v_exp_f32_e64 v168, v168 clamp\n"
    "v_mfma_f32_32x32x16_bf16 v[176:191], a[28:31], a[60:63], v[176:191]\n"
    "s_waitcnt lgkmcnt(2)\n"
    "ds_read_b128 a[60:63], v227 offset:1536\n"
    "v_exp_f32_e64 v169, v169 clamp\n"
    "v_exp_f32_e64 v170, v170 clamp\n"
    "v_exp_f32_e64 v171, v171 clamp\n"
    "v_mfma_f32_16x16x32_bf16 a[84:87], a[64:67], a[68:71], a[84:87]\n"
    "v_exp_f32_e64 v172, v172 clamp\n"
    "v_exp_f32_e64 v173, v173 clamp\n"
    "v_mfma_f32_16x16x32_bf16 a[88:91], a[64:67], a[72:75], a[88:91]\n"
    "v_exp_f32_e64 v174, v174 clamp\n"
    "v_exp_f32_e64 v175, v175 clamp\n"
    "v_pk_fma_f32 v[220:221], v[208:209], v[216:217], v[222:223]\n"
    "v_pk_add_f32 v[160:161], v[160:161], v[192:193]\n"
    "v_pk_add_f32 v[162:163], v[162:163], v[194:195]\n"
    "v_pk_add_f32 v[164:165], v[164:165], v[196:197]\n"
    "v_pk_add_f32 v[166:167], v[166:167], v[198:199]\n"
    "v_pk_fma_f32 v[220:221], v[212:213], v[218:219], v[220:221]\n"
    "v_pk_add_f32 v[168:169], v[168:169], v[200:201]\n"
    "v_pk_add_f32 v[170:171], v[170:171], v[202:203]\n"
    "v_pk_add_f32 v[172:173], v[172:173], v[204:205]\n"
    "v_pk_add_f32 v[174:175], v[174:175], v[206:207]\n"
    "v_pk_add_f32 v[208:209], v[160:161], v[162:163]\n"
    "v_pk_add_f32 v[210:211], v[164:165], v[166:167]\n"
    "v_pk_add_f32 v[212:213], v[168:169], v[170:171]\n"
    "v_pk_add_f32 v[214:215], v[172:173], v[174:175]\n"
    "v_pk_mul_f32 v[160:161], v[160:161], v[162:163]\n"
    "v_pk_mul_f32 v[164:165], v[164:165], v[166:167]\n"
    "v_pk_mul_f32 v[168:169], v[168:169], v[170:171]\n"
    "v_pk_mul_f32 v[172:173], v[172:173], v[174:175]\n"
    "v_pk_mul_f32 v[208:209], v[208:209], v[164:165]\n"
    "v_pk_mul_f32 v[212:213], v[212:213], v[172:173]\n"
    "v_pk_mul_f32 v[216:217], v[160:161], v[164:165]\n"
    "v_pk_mul_f32 v[218:219], v[168:169], v[172:173]\n"
    "v_pk_fma_f32 v[208:209], v[210:211], v[160:161], v[208:209]\n"
    "v_pk_fma_f32 v[212:213], v[214:215], v[168:169], v[212:213]\n"
    "v_mfma_f32_32x32x16_bf16 v[160:175], v[0:3], a[32:35], v[96:111]\n"
    "s_waitcnt vmcnt(4) lgkmcnt(0)\n"
    "s_barrier\n"
    "ds_read_b128 a[32:35], v226 offset:2048\n"
    "v_add_f32 v224, v220, v221\n"
    "v_rcp_f32 v216, v216\n"
    "v_mfma_f32_32x32x16_bf16 v[160:175], v[4:7], a[36:39], v[160:175]\n"
    "ds_read_b128 a[36:39], v227 offset:2048\n"
    "ds_read_b128 a[64:67], v234 offset:192\n"
    "v_cvt_pk_bf16_f32 v224, v224, v224\n"
    "v_rcp_f32 v217, v217\n"
    "v_rcp_f32 v218, v218\n"
    "v_mfma_f32_32x32x16_bf16 v[160:175], v[8:11], a[40:43], v[160:175]\n"
    "ds_read_b128 a[40:43], v226 offset:2560\n"
    "ds_read_b64_tr_b16 a[68:69], v228 offset:26624\n"
    "v_rcp_f32 v219, v219\n"
    "v_exp_f32_e64 v176, v176 clamp\n"
    "v_mfma_f32_32x32x16_bf16 v[160:175], v[12:15], a[44:47], v[160:175]\n"
    "ds_read_b128 a[44:47], v227 offset:2560\n"
    "ds_read_b64_tr_b16 a[70:71], v229 offset:26624\n"
    "v_exp_f32_e64 v177, v177 clamp\n"
    "v_exp_f32_e64 v178, v178 clamp\n"
    "v_mfma_f32_32x32x16_bf16 v[160:175], v[16:19], a[48:51], v[160:175]\n"
    "ds_read_b128 a[48:51], v226 offset:3072\n"
    "ds_read_b64_tr_b16 a[72:73], v230 offset:26624\n"
    "v_exp_f32_e64 v179, v179 clamp\n"
    "v_exp_f32_e64 v180, v180 clamp\n"
    "v_mfma_f32_32x32x16_bf16 v[160:175], v[20:23], a[52:55], v[160:175]\n"
    "ds_read_b128 a[52:55], v227 offset:3072\n"
    "ds_read_b64_tr_b16 a[74:75], v231 offset:26624\n"
    "v_exp_f32_e64 v181, v181 clamp\n"
    "v_exp_f32_e64 v182, v182 clamp\n"
    "v_mfma_f32_32x32x16_bf16 v[160:175], v[24:27], a[56:59], v[160:175]\n"
    "ds_read_b128 a[56:59], v226 offset:3584\n"
    "ds_write_b16 v232, v224 offset:41152\n"
    "v_exp_f32_e64 v183, v183 clamp\n"
    "v_exp_f32_e64 v184, v184 clamp\n"
    "v_mfma_f32_32x32x16_bf16 v[160:175], v[28:31], a[60:63], v[160:175]\n"
    "s_waitcnt lgkmcnt(2)\n"
    "ds_read_b128 a[60:63], v227 offset:3584\n"
    "v_exp_f32_e64 v185, v185 clamp\n"
    "v_exp_f32_e64 v186, v186 clamp\n"
    "v_exp_f32_e64 v187, v187 clamp\n"
    "v_mfma_f32_16x16x32_bf16 a[76:79], a[64:67], a[68:71], a[76:79]\n"
    "v_exp_f32_e64 v188, v188 clamp\n"
    "v_exp_f32_e64 v189, v189 clamp\n"
    "v_mfma_f32_16x16x32_bf16 a[80:83], a[64:67], a[72:75], a[80:83]\n"
    "v_exp_f32_e64 v190, v190 clamp\n"
    "v_exp_f32_e64 v191, v191 clamp\n"
    "v_pk_fma_f32 v[220:221], v[208:209], v[216:217], v[222:223]\n"
    "v_pk_add_f32 v[176:177], v[176:177], v[192:193]\n"
    "v_pk_add_f32 v[178:179], v[178:179], v[194:195]\n"
    "v_pk_add_f32 v[180:181], v[180:181], v[196:197]\n"
    "v_pk_add_f32 v[182:183], v[182:183], v[198:199]\n"
    "v_pk_fma_f32 v[220:221], v[212:213], v[218:219], v[220:221]\n"
    "v_pk_add_f32 v[184:185], v[184:185], v[200:201]\n"
    "v_pk_add_f32 v[186:187], v[186:187], v[202:203]\n"
    "v_pk_add_f32 v[188:189], v[188:189], v[204:205]\n"
    "v_pk_add_f32 v[190:191], v[190:191], v[206:207]\n"
    "v_pk_add_f32 v[208:209], v[176:177], v[178:179]\n"
    "v_pk_add_f32 v[210:211], v[180:181], v[182:183]\n"
    "v_pk_add_f32 v[212:213], v[184:185], v[186:187]\n"
    "v_pk_add_f32 v[214:215], v[188:189], v[190:191]\n"
    "v_pk_mul_f32 v[176:177], v[176:177], v[178:179]\n"
    "v_pk_mul_f32 v[180:181], v[180:181], v[182:183]\n"
    "v_pk_mul_f32 v[184:185], v[184:185], v[186:187]\n"
    "v_pk_mul_f32 v[188:189], v[188:189], v[190:191]\n"
    "v_pk_mul_f32 v[208:209], v[208:209], v[180:181]\n"
    "v_pk_mul_f32 v[212:213], v[212:213], v[188:189]\n"
    "v_pk_mul_f32 v[216:217], v[176:177], v[180:181]\n"
    "v_pk_mul_f32 v[218:219], v[184:185], v[188:189]\n"
    "v_pk_fma_f32 v[208:209], v[210:211], v[176:177], v[208:209]\n"
    "v_pk_fma_f32 v[212:213], v[214:215], v[184:185], v[212:213]\n"
    "v_mfma_f32_32x32x16_bf16 v[176:191], v[32:35], a[32:35], v[112:127]\n"
    "s_waitcnt vmcnt(2) lgkmcnt(0)\n"
    "s_barrier\n"
    "ds_read_b128 a[32:35], v226 offset:4096\n"
    "v_add_f32 v224, v220, v221\n"
    "v_rcp_f32 v216, v216\n"
    "v_mfma_f32_32x32x16_bf16 v[176:191], v[36:39], a[36:39], v[176:191]\n"
    "ds_read_b128 a[36:39], v227 offset:4096\n"
    "ds_read_b128 a[64:67], v235 offset:192\n"
    "v_cvt_pk_bf16_f32 v224, v224, v224\n"
    "v_rcp_f32 v217, v217\n"
    "v_rcp_f32 v218, v218\n"
    "v_mfma_f32_32x32x16_bf16 v[176:191], v[40:43], a[40:43], v[176:191]\n"
    "ds_read_b128 a[40:43], v226 offset:4608\n"
    "ds_read_b64_tr_b16 a[68:69], v228 offset:28672\n"
    "v_rcp_f32 v219, v219\n"
    "v_exp_f32_e64 v160, v160 clamp\n"
    "v_mfma_f32_32x32x16_bf16 v[176:191], v[44:47], a[44:47], v[176:191]\n"
    "ds_read_b128 a[44:47], v227 offset:4608\n"
    "ds_read_b64_tr_b16 a[70:71], v229 offset:28672\n"
    "v_exp_f32_e64 v161, v161 clamp\n"
    "v_exp_f32_e64 v162, v162 clamp\n"
    "v_mfma_f32_32x32x16_bf16 v[176:191], v[48:51], a[48:51], v[176:191]\n"
    "ds_read_b128 a[48:51], v226 offset:5120\n"
    "ds_read_b64_tr_b16 a[72:73], v230 offset:28672\n"
    "v_exp_f32_e64 v163, v163 clamp\n"
    "v_exp_f32_e64 v164, v164 clamp\n"
    "v_mfma_f32_32x32x16_bf16 v[176:191], v[52:55], a[52:55], v[176:191]\n"
    "ds_read_b128 a[52:55], v227 offset:5120\n"
    "ds_read_b64_tr_b16 a[74:75], v231 offset:28672\n"
    "v_exp_f32_e64 v165, v165 clamp\n"
    "v_exp_f32_e64 v166, v166 clamp\n"
    "v_mfma_f32_32x32x16_bf16 v[176:191], v[56:59], a[56:59], v[176:191]\n"
    "ds_read_b128 a[56:59], v226 offset:5632\n"
    "ds_write_b16 v233, v224 offset:192\n"
    "v_exp_f32_e64 v167, v167 clamp\n"
    "v_exp_f32_e64 v168, v168 clamp\n"
    "v_mfma_f32_32x32x16_bf16 v[176:191], v[60:63], a[60:63], v[176:191]\n"
    "s_waitcnt lgkmcnt(2)\n"
    "ds_read_b128 a[60:63], v227 offset:5632\n"
    "v_exp_f32_e64 v169, v169 clamp\n"
    "v_exp_f32_e64 v170, v170 clamp\n"
    "v_exp_f32_e64 v171, v171 clamp\n"
    "v_mfma_f32_16x16x32_bf16 a[76:79], a[64:67], a[68:71], a[76:79]\n"
    "v_exp_f32_e64 v172, v172 clamp\n"
    "v_exp_f32_e64 v173, v173 clamp\n"
    "v_mfma_f32_16x16x32_bf16 a[80:83], a[64:67], a[72:75], a[80:83]\n"
    "v_exp_f32_e64 v174, v174 clamp\n"
    "v_exp_f32_e64 v175, v175 clamp\n"
    "v_pk_fma_f32 v[220:221], v[208:209], v[216:217], v[222:223]\n"
    "v_pk_add_f32 v[160:161], v[160:161], v[192:193]\n"
    "v_pk_add_f32 v[162:163], v[162:163], v[194:195]\n"
    "v_pk_add_f32 v[164:165], v[164:165], v[196:197]\n"
    "v_pk_add_f32 v[166:167], v[166:167], v[198:199]\n"
    "v_pk_fma_f32 v[220:221], v[212:213], v[218:219], v[220:221]\n"
    "v_pk_add_f32 v[168:169], v[168:169], v[200:201]\n"
    "v_pk_add_f32 v[170:171], v[170:171], v[202:203]\n"
    "v_pk_add_f32 v[172:173], v[172:173], v[204:205]\n"
    "v_pk_add_f32 v[174:175], v[174:175], v[206:207]\n"
    "v_pk_add_f32 v[208:209], v[160:161], v[162:163]\n"
    "v_pk_add_f32 v[210:211], v[164:165], v[166:167]\n"
    "v_pk_add_f32 v[212:213], v[168:169], v[170:171]\n"
    "v_pk_add_f32 v[214:215], v[172:173], v[174:175]\n"
    "v_pk_mul_f32 v[160:161], v[160:161], v[162:163]\n"
    "v_pk_mul_f32 v[164:165], v[164:165], v[166:167]\n"
    "v_pk_mul_f32 v[168:169], v[168:169], v[170:171]\n"
    "v_pk_mul_f32 v[172:173], v[172:173], v[174:175]\n"
    "v_pk_mul_f32 v[208:209], v[208:209], v[164:165]\n"
    "v_pk_mul_f32 v[212:213], v[212:213], v[172:173]\n"
    "v_pk_mul_f32 v[216:217], v[160:161], v[164:165]\n"
    "v_pk_mul_f32 v[218:219], v[168:169], v[172:173]\n"
    "v_pk_fma_f32 v[208:209], v[210:211], v[160:161], v[208:209]\n"
    "v_pk_fma_f32 v[212:213], v[214:215], v[168:169], v[212:213]\n"
    "v_mfma_f32_32x32x16_bf16 v[160:175], v[64:67], a[32:35], v[128:143]\n"
    "s_waitcnt vmcnt(0) lgkmcnt(0)\n"
    "s_barrier\n"
    "ds_read_b128 a[32:35], v226 offset:8192\n"
    "v_add_f32 v224, v220, v221\n"
    "v_rcp_f32 v216, v216\n"
    "v_mfma_f32_32x32x16_bf16 v[160:175], v[68:71], a[36:39], v[160:175]\n"
    "ds_read_b128 a[36:39], v227 offset:8192\n"
    "ds_read_b128 a[64:67], v236 offset:192\n"
    "v_cvt_pk_bf16_f32 v224, v224, v224\n"
    "v_rcp_f32 v217, v217\n"
    "v_rcp_f32 v218, v218\n"
    "v_mfma_f32_32x32x16_bf16 v[160:175], v[72:75], a[40:43], v[160:175]\n"
    "ds_read_b128 a[40:43], v226 offset:8704\n"
    "ds_read_b64_tr_b16 a[68:69], v228 offset:30720\n"
    "v_rcp_f32 v219, v219\n"
    "v_exp_f32_e64 v176, v176 clamp\n"
    "v_mfma_f32_32x32x16_bf16 v[160:175], v[76:79], a[44:47], v[160:175]\n"
    "ds_read_b128 a[44:47], v227 offset:8704\n"
    "ds_read_b64_tr_b16 a[70:71], v229 offset:30720\n"
    "v_exp_f32_e64 v177, v177 clamp\n"
    "v_exp_f32_e64 v178, v178 clamp\n"
    "v_mfma_f32_32x32x16_bf16 v[160:175], v[80:83], a[48:51], v[160:175]\n"
    "ds_read_b128 a[48:51], v226 offset:9216\n"
    "ds_read_b64_tr_b16 a[72:73], v230 offset:30720\n"
    "v_exp_f32_e64 v179, v179 clamp\n"
    "v_exp_f32_e64 v180, v180 clamp\n"
    "v_mfma_f32_32x32x16_bf16 v[160:175], v[84:87], a[52:55], v[160:175]\n"
    "ds_read_b128 a[52:55], v227 offset:9216\n"
    "ds_read_b64_tr_b16 a[74:75], v231 offset:30720\n"
    "v_exp_f32_e64 v181, v181 clamp\n"
    "v_exp_f32_e64 v182, v182 clamp\n"
    "v_mfma_f32_32x32x16_bf16 v[160:175], v[88:91], a[56:59], v[160:175]\n"
    "ds_read_b128 a[56:59], v226 offset:9728\n"
    "ds_write_b16 v233, v224 offset:41152\n"
    "v_exp_f32_e64 v183, v183 clamp\n"
    "v_exp_f32_e64 v184, v184 clamp\n"
    "v_mfma_f32_32x32x16_bf16 v[160:175], v[92:95], a[60:63], v[160:175]\n"
    "s_waitcnt lgkmcnt(2)\n"
    "ds_read_b128 a[60:63], v227 offset:9728\n"
    "v_exp_f32_e64 v185, v185 clamp\n"
    "v_exp_f32_e64 v186, v186 clamp\n"
    "v_exp_f32_e64 v187, v187 clamp\n"
    "v_mfma_f32_16x16x32_bf16 a[84:87], a[64:67], a[68:71], a[84:87]\n"
    "v_exp_f32_e64 v188, v188 clamp\n"
    "v_exp_f32_e64 v189, v189 clamp\n"
    "v_mfma_f32_16x16x32_bf16 a[88:91], a[64:67], a[72:75], a[88:91]\n"
    "v_exp_f32_e64 v190, v190 clamp\n"
    "v_exp_f32_e64 v191, v191 clamp\n"
    "v_pk_fma_f32 v[220:221], v[208:209], v[216:217], v[222:223]\n"
    "v_pk_add_f32 v[176:177], v[176:177], v[192:193]\n"
    "v_pk_add_f32 v[178:179], v[178:179], v[194:195]\n"
    "v_pk_add_f32 v[180:181], v[180:181], v[196:197]\n"
    "v_pk_add_f32 v[182:183], v[182:183], v[198:199]\n"
    "v_pk_fma_f32 v[220:221], v[212:213], v[218:219], v[220:221]\n"
    "v_pk_add_f32 v[184:185], v[184:185], v[200:201]\n"
    "v_pk_add_f32 v[186:187], v[186:187], v[202:203]\n"
    "v_pk_add_f32 v[188:189], v[188:189], v[204:205]\n"
    "v_pk_add_f32 v[190:191], v[190:191], v[206:207]\n"
    "v_pk_add_f32 v[208:209], v[176:177], v[178:179]\n"
    "v_pk_add_f32 v[210:211], v[180:181], v[182:183]\n"
    "v_pk_add_f32 v[212:213], v[184:185], v[186:187]\n"
    "v_pk_add_f32 v[214:215], v[188:189], v[190:191]\n"
    "v_pk_mul_f32 v[176:177], v[176:177], v[178:179]\n"
    "v_pk_mul_f32 v[180:181], v[180:181], v[182:183]\n"
    "v_pk_mul_f32 v[184:185], v[184:185], v[186:187]\n"
    "v_pk_mul_f32 v[188:189], v[188:189], v[190:191]\n"
    "v_pk_mul_f32 v[208:209], v[208:209], v[180:181]\n"
    "v_pk_mul_f32 v[212:213], v[212:213], v[188:189]\n"
    "v_pk_mul_f32 v[216:217], v[176:177], v[180:181]\n"
    "v_pk_mul_f32 v[218:219], v[184:185], v[188:189]\n"
    "v_pk_fma_f32 v[208:209], v[210:211], v[176:177], v[208:209]\n"
    "v_pk_fma_f32 v[212:213], v[214:215], v[184:185], v[212:213]\n"
    "v_mfma_f32_32x32x16_bf16 v[176:191], a[0:3], a[32:35], v[144:159]\n"
    "s_waitcnt lgkmcnt(0)\n"
    "s_barrier\n"
    "v_add_f32 v224, v220, v221\n"
    "v_rcp_f32 v216, v216\n"
    "v_mfma_f32_32x32x16_bf16 v[176:191], a[4:7], a[36:39], v[176:191]\n"
    "ds_read_b128 a[64:67], v237 offset:192\n"
    "v_cvt_pk_bf16_f32 v224, v224, v224\n"
    "v_rcp_f32 v217, v217\n"
    "v_rcp_f32 v218, v218\n"
    "v_mfma_f32_32x32x16_bf16 v[176:191], a[8:11], a[40:43], v[176:191]\n"
    "ds_read_b64_tr_b16 a[68:69], v228 offset:6144\n"
    "v_rcp_f32 v219, v219\n"
    "v_exp_f32_e64 v160, v160 clamp\n"
    "v_mfma_f32_32x32x16_bf16 v[176:191], a[12:15], a[44:47], v[176:191]\n"
    "ds_read_b64_tr_b16 a[70:71], v229 offset:6144\n"
    "v_exp_f32_e64 v161, v161 clamp\n"
    "v_exp_f32_e64 v162, v162 clamp\n"
    "v_mfma_f32_32x32x16_bf16 v[176:191], a[16:19], a[48:51], v[176:191]\n"
    "ds_read_b64_tr_b16 a[72:73], v230 offset:6144\n"
    "v_exp_f32_e64 v163, v163 clamp\n"
    "v_exp_f32_e64 v164, v164 clamp\n"
    "v_mfma_f32_32x32x16_bf16 v[176:191], a[20:23], a[52:55], v[176:191]\n"
    "ds_read_b64_tr_b16 a[74:75], v231 offset:6144\n"
    "v_exp_f32_e64 v165, v165 clamp\n"
    "v_exp_f32_e64 v166, v166 clamp\n"
    "v_mfma_f32_32x32x16_bf16 v[176:191], a[24:27], a[56:59], v[176:191]\n"
    "ds_write_b16 v232, v224 offset:0\n"
    "v_exp_f32_e64 v167, v167 clamp\n"
    "v_exp_f32_e64 v168, v168 clamp\n"
    "v_mfma_f32_32x32x16_bf16 v[176:191], a[28:31], a[60:63], v[176:191]\n"
    "s_waitcnt lgkmcnt(1)\n"
    "v_exp_f32_e64 v169, v169 clamp\n"
    "v_exp_f32_e64 v170, v170 clamp\n"
    "v_exp_f32_e64 v171, v171 clamp\n"
    "v_mfma_f32_16x16x32_bf16 a[84:87], a[64:67], a[68:71], a[84:87]\n"
    "v_exp_f32_e64 v172, v172 clamp\n"
    "v_exp_f32_e64 v173, v173 clamp\n"
    "v_mfma_f32_16x16x32_bf16 a[88:91], a[64:67], a[72:75], a[88:91]\n"
    "v_exp_f32_e64 v174, v174 clamp\n"
    "v_exp_f32_e64 v175, v175 clamp\n"
    "v_pk_fma_f32 v[220:221], v[208:209], v[216:217], v[222:223]\n"
    "v_pk_add_f32 v[160:161], v[160:161], v[192:193]\n"
    "v_pk_add_f32 v[162:163], v[162:163], v[194:195]\n"
    "v_pk_add_f32 v[164:165], v[164:165], v[196:197]\n"
    "v_pk_add_f32 v[166:167], v[166:167], v[198:199]\n"
    "v_pk_fma_f32 v[220:221], v[212:213], v[218:219], v[220:221]\n"
    "v_pk_add_f32 v[168:169], v[168:169], v[200:201]\n"
    "v_pk_add_f32 v[170:171], v[170:171], v[202:203]\n"
    "v_pk_add_f32 v[172:173], v[172:173], v[204:205]\n"
    "v_pk_add_f32 v[174:175], v[174:175], v[206:207]\n"
    "v_pk_add_f32 v[208:209], v[160:161], v[162:163]\n"
    "v_pk_add_f32 v[210:211], v[164:165], v[166:167]\n"
    "v_pk_add_f32 v[212:213], v[168:169], v[170:171]\n"
    "v_pk_add_f32 v[214:215], v[172:173], v[174:175]\n"
    "v_pk_mul_f32 v[160:161], v[160:161], v[162:163]\n"
    "v_pk_mul_f32 v[164:165], v[164:165], v[166:167]\n"
    "v_pk_mul_f32 v[168:169], v[168:169], v[170:171]\n"
    "v_pk_mul_f32 v[172:173], v[172:173], v[174:175]\n"
    "v_pk_mul_f32 v[208:209], v[208:209], v[164:165]\n"
    "v_pk_mul_f32 v[212:213], v[212:213], v[172:173]\n"
    "v_pk_mul_f32 v[216:217], v[160:161], v[164:165]\n"
    "v_pk_mul_f32 v[218:219], v[168:169], v[172:173]\n"
    "v_pk_fma_f32 v[208:209], v[210:211], v[160:161], v[208:209]\n"
    "v_pk_fma_f32 v[212:213], v[214:215], v[168:169], v[212:213]\n"
    "v_rcp_f32 v216, v216\n"
    "v_rcp_f32 v217, v217\n"
    "v_rcp_f32 v218, v218\n"
    "v_rcp_f32 v219, v219\n"
    "v_add_f32 v224, v220, v221\n"
    "v_cvt_pk_bf16_f32 v224, v224, v224\n"
    "ds_write_b16 v232, v224 offset:40960\n"
    "ds_read_b64_tr_b16 a[36:37], v228 offset:0\n"
    "ds_read_b64_tr_b16 a[38:39], v229 offset:0\n"
    "v_exp_f32_e64 v176, v176 clamp\n"
    "v_exp_f32_e64 v177, v177 clamp\n"
    "v_exp_f32_e64 v178, v178 clamp\n"
    "v_exp_f32_e64 v179, v179 clamp\n"
    "v_exp_f32_e64 v180, v180 clamp\n"
    "v_exp_f32_e64 v181, v181 clamp\n"
    "v_exp_f32_e64 v182, v182 clamp\n"
    "v_exp_f32_e64 v183, v183 clamp\n"
    "v_exp_f32_e64 v184, v184 clamp\n"
    "v_exp_f32_e64 v185, v185 clamp\n"
    "v_exp_f32_e64 v186, v186 clamp\n"
    "v_exp_f32_e64 v187, v187 clamp\n"
    "v_exp_f32_e64 v188, v188 clamp\n"
    "v_exp_f32_e64 v189, v189 clamp\n"
    "v_exp_f32_e64 v190, v190 clamp\n"
    "v_exp_f32_e64 v191, v191 clamp\n"
    "v_pk_fma_f32 v[220:221], v[208:209], v[216:217], v[222:223]\n"
    "v_pk_add_f32 v[176:177], v[176:177], v[192:193]\n"
    "v_pk_add_f32 v[178:179], v[178:179], v[194:195]\n"
    "v_pk_add_f32 v[180:181], v[180:181], v[196:197]\n"
    "v_pk_add_f32 v[182:183], v[182:183], v[198:199]\n"
    "v_pk_fma_f32 v[220:221], v[212:213], v[218:219], v[220:221]\n"
    "v_pk_add_f32 v[184:185], v[184:185], v[200:201]\n"
    "v_pk_add_f32 v[186:187], v[186:187], v[202:203]\n"
    "v_pk_add_f32 v[188:189], v[188:189], v[204:205]\n"
    "v_pk_add_f32 v[190:191], v[190:191], v[206:207]\n"
    "v_pk_add_f32 v[208:209], v[176:177], v[178:179]\n"
    "v_pk_add_f32 v[210:211], v[180:181], v[182:183]\n"
    "v_pk_add_f32 v[212:213], v[184:185], v[186:187]\n"
    "v_pk_add_f32 v[214:215], v[188:189], v[190:191]\n"
    "v_pk_mul_f32 v[176:177], v[176:177], v[178:179]\n"
    "v_pk_mul_f32 v[180:181], v[180:181], v[182:183]\n"
    "v_pk_mul_f32 v[184:185], v[184:185], v[186:187]\n"
    "v_pk_mul_f32 v[188:189], v[188:189], v[190:191]\n"
    "v_pk_mul_f32 v[208:209], v[208:209], v[180:181]\n"
    "v_pk_mul_f32 v[212:213], v[212:213], v[188:189]\n"
    "v_pk_mul_f32 v[216:217], v[176:177], v[180:181]\n"
    "v_pk_mul_f32 v[218:219], v[184:185], v[188:189]\n"
    "v_pk_fma_f32 v[208:209], v[210:211], v[176:177], v[208:209]\n"
    "v_pk_fma_f32 v[212:213], v[214:215], v[184:185], v[212:213]\n"
    "v_rcp_f32 v216, v216\n"
    "v_rcp_f32 v217, v217\n"
    "v_add_f32 v224, v220, v221\n"
    "v_cvt_pk_bf16_f32 v224, v224, v224\n"
    "v_rcp_f32 v218, v218\n"
    "v_rcp_f32 v219, v219\n"
    "ds_write_b16 v233, v224 offset:0\n"
    "ds_read_b64_tr_b16 a[40:41], v230 offset:0\n"
    "ds_read_b64_tr_b16 a[42:43], v231 offset:0\n"
    "v_pk_fma_f32 v[220:221], v[208:209], v[216:217], v[222:223]\n"
    "s_nop 0\n"
    "v_pk_fma_f32 v[220:221], v[212:213], v[218:219], v[220:221]\n"
    "v_add_f32 v224, v220, v221\n"
    "v_cvt_pk_bf16_f32 v224, v224, v224\n"
    "ds_write_b16 v233, v224 offset:40960\n"
    "ds_read_b64_tr_b16 a[48:49], v228 offset:2048\n"
    "ds_read_b64_tr_b16 a[50:51], v229 offset:2048\n"
    "ds_read_b64_tr_b16 a[52:53], v230 offset:2048\n"
    "ds_read_b64_tr_b16 a[54:55], v231 offset:2048\n"
    "ds_read_b64_tr_b16 a[128:129], v228 offset:4096\n"
    "ds_read_b64_tr_b16 a[130:131], v229 offset:4096\n"
    "ds_read_b64_tr_b16 a[132:133], v230 offset:4096\n"
    "ds_read_b64_tr_b16 a[134:135], v231 offset:4096\n"
    "s_waitcnt lgkmcnt(8)\n"
    "s_barrier\n"
    "ds_read_b128 a[32:35], v234 offset:0\n"
    "ds_read_b128 a[44:47], v235 offset:0\n"
    "ds_read_b128 a[124:127], v236 offset:0\n"
    "ds_read_b128 a[136:139], v237 offset:0\n"
    "ds_read_b64_tr_b16 a[140:141], v228 offset:8192\n"
    "ds_read_b64_tr_b16 a[142:143], v229 offset:8192\n"
    "ds_read_b64_tr_b16 a[144:145], v230 offset:8192\n"
    "ds_read_b64_tr_b16 a[146:147], v231 offset:8192\n"
    "v_accvgpr_read_b32 v96, a76\n"
    "v_accvgpr_read_b32 v97, a77\n"
    "v_accvgpr_read_b32 v98, a78\n"
    "v_accvgpr_read_b32 v99, a79\n"
    "v_accvgpr_read_b32 v100, a80\n"
    "v_accvgpr_read_b32 v101, a81\n"
    "v_accvgpr_read_b32 v102, a82\n"
    "v_accvgpr_read_b32 v103, a83\n"
    "v_accvgpr_read_b32 v104, a84\n"
    "v_accvgpr_read_b32 v105, a85\n"
    "v_accvgpr_read_b32 v106, a86\n"
    "v_accvgpr_read_b32 v107, a87\n"
    "v_accvgpr_read_b32 v108, a88\n"
    "v_accvgpr_read_b32 v109, a89\n"
    "v_accvgpr_read_b32 v110, a90\n"
    "v_accvgpr_read_b32 v111, a91\n"
    "s_waitcnt lgkmcnt(7)\n"
    "v_mfma_f32_16x16x32_bf16 v[96:99], a[32:35], a[36:39], v[96:99]\n"
    "v_mfma_f32_16x16x32_bf16 v[100:103], a[32:35], a[40:43], v[100:103]\n"
    "s_waitcnt lgkmcnt(6)\n"
    "v_mfma_f32_16x16x32_bf16 v[96:99], a[44:47], a[48:51], v[96:99]\n"
    "v_mfma_f32_16x16x32_bf16 v[100:103], a[44:47], a[52:55], v[100:103]\n"
    "s_waitcnt lgkmcnt(5)\n"
    "v_mfma_f32_16x16x32_bf16 v[104:107], a[124:127], a[128:131], v[104:107]\n"
    "v_mfma_f32_16x16x32_bf16 v[108:111], a[124:127], a[132:135], v[108:111]\n"
    "s_waitcnt lgkmcnt(2)\n"
    "v_mfma_f32_16x16x32_bf16 v[104:107], a[136:139], a[140:143], v[104:107]\n"
    "s_waitcnt lgkmcnt(0)\n"
    "v_mfma_f32_16x16x32_bf16 v[108:111], a[136:139], a[144:147], v[108:111]\n"
    "v_pk_add_f32 v[96:97], v[96:97], v[98:99]\n"
    "v_pk_add_f32 v[100:101], v[100:101], v[102:103]\n"
    "s_nop 3\n"
    "v_pk_add_f32 v[104:105], v[104:105], v[106:107]\n"
    "s_nop 0\n"
    "v_pk_add_f32 v[108:109], v[108:109], v[110:111]\n"
    "v_add_f32 v96, v96, v97\n"
    "v_add_f32 v100, v100, v101\n"
    "v_add_f32 v104, v104, v105\n"
    "v_add_f32 v108, v108, v109\n"
    "v_permlane16_swap_b32 v96, v100\n"
    "s_nop 0\n"
    "v_permlane16_swap_b32 v104, v108\n"
    "v_add_f32 v96, v96, v100\n"
    "v_add_f32 v104, v104, v108\n"
    "v_mul_f32 v96, s64, v96\n"
    "v_mul_f32 v104, s64, v104\n"
    "v_cvt_pk_bf16_f32 v96, v96, v104\n"
    "buffer_store_short v96, v240, s[40:43], 0 offen\n"
    "buffer_store_short_d16_hi v96, v240, s[40:43], 0 offen offset:512\n"
    "s_endpgm\n"
    "L_gen_%=:\n"
    "v_mov_b32 v223, 0\n"
    "v_mov_b32 v222, v198\n"
    "ds_read_b128 v[192:195], v255 offset:7488\n"
    "ds_read_b128 v[196:199], v255 offset:7520\n"
    "ds_read_b128 v[200:203], v255 offset:7552\n"
    "ds_read_b128 v[204:207], v255 offset:7584\n"
    "v_cvt_pk_bf16_f32 v140, v140, v141\n"
    "v_cvt_pk_bf16_f32 v142, v142, v143\n"
    "v_cvt_pk_bf16_f32 v156, v156, v157\n"
    "v_cvt_pk_bf16_f32 v158, v158, v159\n"
    "v_cvt_pk_bf16_f32 v28, v128, v129\n"
    "v_cvt_pk_bf16_f32 v29, v130, v131\n"
    "v_cvt_pk_bf16_f32 v30, v144, v145\n"
    "v_cvt_pk_bf16_f32 v31, v146, v147\n"
    "v_cvt_pk_bf16_f32 v60, v132, v133\n"
    "v_cvt_pk_bf16_f32 v61, v134, v135\n"
    "v_cvt_pk_bf16_f32 v62, v148, v149\n"
    "v_cvt_pk_bf16_f32 v63, v150, v151\n"
    "v_cvt_pk_bf16_f32 v92, v136, v137\n"
    "v_cvt_pk_bf16_f32 v93, v138, v139\n"
    "v_cvt_pk_bf16_f32 v94, v152, v153\n"
    "v_cvt_pk_bf16_f32 v95, v154, v155\n"
    "v_accvgpr_write_b32 a28, v140\n"
    "v_accvgpr_write_b32 a29, v142\n"
    "v_accvgpr_write_b32 a30, v156\n"
    "v_accvgpr_write_b32 a31, v158\n"
    "ds_read_b128 v[112:115], v239 offset:3200\n"
    "ds_read_b128 v[116:119], v239 offset:3232\n"
    "ds_read_b128 v[120:123], v239 offset:3264\n"
    "ds_read_b128 v[124:127], v239 offset:3296\n"
    "ds_read_b128 v[128:131], v239 offset:3328\n"
    "ds_read_b128 v[132:135], v239 offset:3360\n"
    "ds_read_b128 v[136:139], v239 offset:3392\n"
    "ds_read_b128 v[140:143], v239 offset:3424\n"
    "ds_read_b128 v[144:147], v239 offset:3456\n"
    "ds_read_b128 v[148:151], v239 offset:3488\n"
    "ds_read_b128 v[152:155], v239 offset:3520\n"
    "ds_read_b128 v[156:159], v239 offset:3552\n"
    "ds_write_b32 v253, v223 offset:2176\n"
    "v_accvgpr_write_b32 a76, 0\n"
    "v_accvgpr_write_b32 a77, 0\n"
    "v_accvgpr_write_b32 a78, 0\n"
    "v_accvgpr_write_b32 a79, 0\n"
    "v_accvgpr_write_b32 a80, 0\n"
    "v_accvgpr_write_b32 a81, 0\n"
    "v_accvgpr_write_b32 a82, 0\n"
    "v_accvgpr_write_b32 a83, 0\n"
    "v_accvgpr_write_b32 a84, 0\n"
    "v_accvgpr_write_b32 a85, 0\n"
    "v_accvgpr_write_b32 a86, 0\n"
    "v_accvgpr_write_b32 a87, 0\n"
    "v_accvgpr_write_b32 a88, 0\n"
    "v_accvgpr_write_b32 a89, 0\n"
    "v_accvgpr_write_b32 a90, 0\n"
    "v_accvgpr_write_b32 a91, 0\n"
    "ds_write_b32 v253, v223 offset:2560\n"
    "ds_write_b32 v253, v223 offset:2816\n"
    "v_mbcnt_lo_u32_b32 v160, s70, 0\n"
    "v_mbcnt_hi_u32_b32 v160, s71, v160\n"
    "v_mbcnt_lo_u32_b32 v160, s72, v160\n"
    "v_mbcnt_hi_u32_b32 v160, s73, v160\n"
    "v_mbcnt_lo_u32_b32 v160, s74, v160\n"
    "v_mbcnt_hi_u32_b32 v160, s75, v160\n"
    "v_mbcnt_lo_u32_b32 v160, s76, v160\n"
    "v_mbcnt_hi_u32_b32 v160, s77, v160\n"
    "v_addc_co_u32 v161, vcc, 0, v160, s[70:71]\n"
    "v_addc_co_u32 v162, vcc, 0, v161, s[72:73]\n"
    "v_addc_co_u32 v163, vcc, 0, v162, s[74:75]\n"
    "v_lshl_add_u32 v160, v160, 1, s81\n"
    "v_lshl_add_u32 v161, v161, 1, s81\n"
    "v_lshl_add_u32 v162, v162, 1, s81\n"
    "v_lshl_add_u32 v163, v163, 1, s81\n"
    "s_mov_b64 exec, s[70:71]\n"
    "ds_write_b16 v160, v224 offset:2560\n"
    "s_mov_b64 exec, s[72:73]\n"
    "ds_write_b16_d16_hi v161, v224 offset:2560\n"
    "s_mov_b64 exec, s[74:75]\n"
    "ds_write_b16 v162, v225 offset:2560\n"
    "s_mov_b64 exec, s[76:77]\n"
    "ds_write_b16_d16_hi v163, v225 offset:2560\n"
    "s_mov_b64 exec, -1\n"
    "v_bfe_u32 v160, v254, 2, 3\n"
    "s_lshl_b32 s47, s44, 6\n"
    "v_lshl_add_u32 v160, v160, 3, s47\n"
    "v_add_u32 v161, 0x14000, v160\n"
    "ds_read_b32 a156, v160 offset:7684\n"
    "ds_read_b32 a157, v160 offset:48644\n"
    "ds_read_b32 a158, v161 offset:7684\n"
    "ds_read_b32 a159, v161 offset:48644\n"
    "s_waitcnt lgkmcnt(0)\n"
    "v_and_b32 v238, 31, v254\n"
    "v_lshlrev_b32 v238, 1, v238\n"
    "v_add_u32 v238, 0x8a00, v238\n"
    "v_add_u32 v239, 0x14000, v238\n"
    "v_mov_b32 v225, 0\n"
    "L_main1_%=:\n"
    "s_waitcnt vmcnt(10)\n"
    "s_barrier\n"
    "ds_read_b128 a[32:35], v226 offset:0\n"
    "ds_read_b128 a[36:39], v227 offset:0\n"
    "ds_read_b128 a[40:43], v226 offset:512\n"
    "ds_read_b128 a[44:47], v227 offset:512\n"
    "ds_read_b128 a[48:51], v226 offset:1024\n"
    "ds_read_b128 a[52:55], v227 offset:1024\n"
    "ds_read_b128 a[56:59], v226 offset:1536\n"
    "ds_read_b128 a[60:63], v227 offset:1536\n"
    "s_waitcnt vmcnt(8)\n"
    "s_barrier\n"
    "s_waitcnt lgkmcnt(7)\n"
    "v_mfma_f32_32x32x16_bf16 v[160:175], v[0:3], a[32:35], v[96:111]\n"
    "ds_read_b128 a[32:35], v226 offset:2048\n"
    "s_waitcnt lgkmcnt(7)\n"
    "v_mfma_f32_32x32x16_bf16 v[160:175], v[4:7], a[36:39], v[160:175]\n"
    "ds_read_b128 a[36:39], v227 offset:2048\n"
    "s_waitcnt lgkmcnt(7)\n"
    "v_mfma_f32_32x32x16_bf16 v[160:175], v[8:11], a[40:43], v[160:175]\n"
    "ds_read_b128 a[40:43], v226 offset:2560\n"
    "s_waitcnt lgkmcnt(7)\n"
    "v_mfma_f32_32x32x16_bf16 v[160:175], v[12:15], a[44:47], v[160:175]\n"
    "ds_read_b128 a[44:47], v227 offset:2560\n"
    "s_waitcnt lgkmcnt(7)\n"
    "v_mfma_f32_32x32x16_bf16 v[160:175], v[16:19], a[48:51], v[160:175]\n"
    "ds_read_b128 a[48:51], v226 offset:3072\n"
    "s_waitcnt lgkmcnt(7)\n"
    "v_mfma_f32_32x32x16_bf16 v[160:175], v[20:23], a[52:55], v[160:175]\n"
    "ds_read_b128 a[52:55], v227 offset:3072\n"
    "s_waitcnt lgkmcnt(7)\n"
    "v_mfma_f32_32x32x16_bf16 v[160:175], v[24:27], a[56:59], v[160:175]\n"
    "ds_read_b128 a[56:59], v226 offset:3584\n"
    "s_waitcnt lgkmcnt(7)\n"
    "v_mfma_f32_32x32x16_bf16 v[160:175], v[28:31], a[60:63], v[160:175]\n"
    "ds_read_b128 a[60:63], v227 offset:3584\n"
    "s_waitcnt lgkmcnt(7)\n"
    "v_mfma_f32_32x32x16_bf16 v[176:191], v[32:35], a[32:35], v[112:127]\n"
    "s_waitcnt vmcnt(6) lgkmcnt(0)\n"
    "s_barrier\n"
    "ds_read_b128 a[32:35], v226 offset:4096\n"
    "s_nop 5\n"
    "v_exp_f32_e64 v160, v160 clamp\n"
    "v_mfma_f32_32x32x16_bf16 v[176:191], v[36:39], a[36:39], v[176:191]\n"
    "ds_read_b128 a[36:39], v227 offset:4096\n"
    "v_exp_f32_e64 v161, v161 clamp\n"
    "v_exp_f32_e64 v162, v162 clamp\n"
    "v_mfma_f32_32x32x16_bf16 v[176:191], v[40:43], a[40:43], v[176:191]\n"
    "ds_read_b128 a[40:43], v226 offset:4608\n"
    "v_exp_f32_e64 v163, v163 clamp\n"
    "v_exp_f32_e64 v164, v164 clamp\n"
    "v_mfma_f32_32x32x16_bf16 v[176:191], v[44:47], a[44:47], v[176:191]\n"
    "ds_read_b128 a[44:47], v227 offset:4608\n"
    "v_exp_f32_e64 v165, v165 clamp\n"
    "v_exp_f32_e64 v166, v166 clamp\n"
    "v_mfma_f32_32x32x16_bf16 v[176:191], v[48:51], a[48:51], v[176:191]\n"
    "ds_read_b128 a[48:51], v226 offset:5120\n"
    "v_perm_b32 v246, v244, v241, s63\n"
    "v_perm_b32 v247, v245, v241, s63\n"
    "v_exp_f32_e64 v167, v167 clamp\n"
    "v_exp_f32_e64 v168, v168 clamp\n"
    "v_mfma_f32_32x32x16_bf16 v[176:191], v[52:55], a[52:55], v[176:191]\n"
    "s_add_u32 m0, s80, 0x3000\n"
    "ds_read_b128 a[52:55], v227 offset:5120\n"
    "v_exp_f32_e64 v169, v169 clamp\n"
    "v_exp_f32_e64 v170, v170 clamp\n"
    "v_mfma_f32_32x32x16_bf16 v[176:191], v[56:59], a[56:59], v[176:191]\n"
    "buffer_load_dwordx4 v246, s[92:95], s50 offen offset:2048 nt lds\n"
    "buffer_load_dwordx4 v246, s[92:95], s51 offen offset:3072 nt lds\n"
    "ds_read_b128 a[56:59], v226 offset:5632\n"
    "v_exp_f32_e64 v171, v171 clamp\n"
    "v_exp_f32_e64 v172, v172 clamp\n"
    "v_mfma_f32_32x32x16_bf16 v[176:191], v[60:63], a[60:63], v[176:191]\n"
    "s_add_u32 m0, s80, 0x4000\n"
    "s_nop 0\n"
    "buffer_load_dwordx4 v247, s[96:99], s48 offen offset:0 nt lds\n"
    "buffer_load_dwordx4 v247, s[96:99], s49 offen offset:1024 nt lds\n"
    "ds_read_b128 a[60:63], v227 offset:5632\n"
    "v_exp_f32_e64 v173, v173 clamp\n"
    "v_exp_f32_e64 v174, v174 clamp\n"
    "v_exp_f32_e64 v175, v175 clamp\n"
    "v_pk_add_f32 v[160:161], v[160:161], v[192:193]\n"
    "v_pk_add_f32 v[162:163], v[162:163], v[194:195]\n"
    "v_pk_add_f32 v[164:165], v[164:165], v[196:197]\n"
    "v_pk_add_f32 v[166:167], v[166:167], v[198:199]\n"
    "v_pk_add_f32 v[168:169], v[168:169], v[200:201]\n"
    "v_pk_add_f32 v[170:171], v[170:171], v[202:203]\n"
    "v_pk_add_f32 v[172:173], v[172:173], v[204:205]\n"
    "v_pk_add_f32 v[174:175], v[174:175], v[206:207]\n"
    "v_pk_add_f32 v[208:209], v[160:161], v[162:163]\n"
    "v_pk_add_f32 v[210:211], v[164:165], v[166:167]\n"
    "v_pk_add_f32 v[212:213], v[168:169], v[170:171]\n"
    "v_pk_add_f32 v[214:215], v[172:173], v[174:175]\n"
    "v_pk_mul_f32 v[160:161], v[160:161], v[162:163]\n"
    "v_pk_mul_f32 v[164:165], v[164:165], v[166:167]\n"
    "v_pk_mul_f32 v[168:169], v[168:169], v[170:171]\n"
    "v_pk_mul_f32 v[172:173], v[172:173], v[174:175]\n"
    "v_pk_mul_f32 v[208:209], v[208:209], v[164:165]\n"
    "v_pk_mul_f32 v[212:213], v[212:213], v[172:173]\n"
    "v_pk_mul_f32 v[216:217], v[160:161], v[164:165]\n"
    "v_pk_mul_f32 v[218:219], v[168:169], v[172:173]\n"
    "v_pk_fma_f32 v[208:209], v[210:211], v[160:161], v[208:209]\n"
    "v_pk_fma_f32 v[212:213], v[214:215], v[168:169], v[212:213]\n"
    "s_waitcnt lgkmcnt(7)\n"
    "v_mfma_f32_32x32x16_bf16 v[160:175], v[64:67], a[32:35], v[128:143]\n"
    "s_waitcnt vmcnt(8) lgkmcnt(0)\n"
    "s_barrier\n"
    "ds_read_b128 a[32:35], v226 offset:8192\n"
    "v_rcp_f32 v216, v216\n"
    "v_mfma_f32_32x32x16_bf16 v[160:175], v[68:71], a[36:39], v[160:175]\n"
    "ds_read_b128 a[36:39], v227 offset:8192\n"
    "v_rcp_f32 v217, v217\n"
    "v_rcp_f32 v218, v218\n"
    "v_mfma_f32_32x32x16_bf16 v[160:175], v[72:75], a[40:43], v[160:175]\n"
    "ds_read_b128 a[40:43], v226 offset:8704\n"
    "v_rcp_f32 v219, v219\n"
    "v_exp_f32_e64 v176, v176 clamp\n"
    "v_mfma_f32_32x32x16_bf16 v[160:175], v[76:79], a[44:47], v[160:175]\n"
    "ds_read_b128 a[44:47], v227 offset:8704\n"
    "v_exp_f32_e64 v177, v177 clamp\n"
    "v_exp_f32_e64 v178, v178 clamp\n"
    "v_mfma_f32_32x32x16_bf16 v[160:175], v[80:83], a[48:51], v[160:175]\n"
    "ds_read_b128 a[48:51], v226 offset:9216\n"
    "v_perm_b32 v246, v242, v241, s61\n"
    "v_exp_f32_e64 v179, v179 clamp\n"
    "v_exp_f32_e64 v180, v180 clamp\n"
    "v_mfma_f32_32x32x16_bf16 v[160:175], v[84:87], a[52:55], v[160:175]\n"
    "ds_read_b128 a[52:55], v227 offset:9216\n"
    "v_exp_f32_e64 v181, v181 clamp\n"
    "v_exp_f32_e64 v182, v182 clamp\n"
    "v_mfma_f32_32x32x16_bf16 v[160:175], v[88:91], a[56:59], v[160:175]\n"
    "buffer_load_dwordx4 v246, s[84:87], s50 offen offset:2048 nt lds\n"
    "buffer_load_dwordx4 v246, s[84:87], s51 offen offset:3072 nt lds\n"
    "ds_read_b128 a[56:59], v226 offset:9728\n"
    "v_exp_f32_e64 v183, v183 clamp\n"
    "v_exp_f32_e64 v184, v184 clamp\n"
    "v_mfma_f32_32x32x16_bf16 v[160:175], v[92:95], a[60:63], v[160:175]\n"
    "ds_read_b128 a[60:63], v227 offset:9728\n"
    "v_exp_f32_e64 v185, v185 clamp\n"
    "v_exp_f32_e64 v186, v186 clamp\n"
    "v_exp_f32_e64 v187, v187 clamp\n"
    "v_exp_f32_e64 v188, v188 clamp\n"
    "v_exp_f32_e64 v189, v189 clamp\n"
    "v_exp_f32_e64 v190, v190 clamp\n"
    "v_exp_f32_e64 v191, v191 clamp\n"
    "v_pk_fma_f32 v[220:221], v[208:209], v[216:217], v[222:223]\n"
    "v_pk_add_f32 v[176:177], v[176:177], v[192:193]\n"
    "v_pk_add_f32 v[178:179], v[178:179], v[194:195]\n"
    "v_pk_add_f32 v[180:181], v[180:181], v[196:197]\n"
    "v_pk_add_f32 v[182:183], v[182:183], v[198:199]\n"
    "v_pk_fma_f32 v[220:221], v[212:213], v[218:219], v[220:221]\n"
    "v_pk_add_f32 v[184:185], v[184:185], v[200:201]\n"
    "v_pk_add_f32 v[186:187], v[186:187], v[202:203]\n"
    "v_pk_add_f32 v[188:189], v[188:189], v[204:205]\n"
    "v_pk_add_f32 v[190:191], v[190:191], v[206:207]\n"
    "v_pk_add_f32 v[208:209], v[176:177], v[178:179]\n"
    "v_pk_add_f32 v[210:211], v[180:181], v[182:183]\n"
    "v_pk_add_f32 v[212:213], v[184:185], v[186:187]\n"
    "v_pk_add_f32 v[214:215], v[188:189], v[190:191]\n"
    "v_pk_mul_f32 v[176:177], v[176:177], v[178:179]\n"
    "v_pk_mul_f32 v[180:181], v[180:181], v[182:183]\n"
    "v_pk_mul_f32 v[184:185], v[184:185], v[186:187]\n"
    "v_pk_mul_f32 v[188:189], v[188:189], v[190:191]\n"
    "v_pk_mul_f32 v[208:209], v[208:209], v[180:181]\n"
    "v_pk_mul_f32 v[212:213], v[212:213], v[188:189]\n"
    "v_pk_mul_f32 v[216:217], v[176:177], v[180:181]\n"
    "v_pk_mul_f32 v[218:219], v[184:185], v[188:189]\n"
    "v_pk_fma_f32 v[208:209], v[210:211], v[176:177], v[208:209]\n"
    "v_pk_fma_f32 v[212:213], v[214:215], v[184:185], v[212:213]\n"
    "s_waitcnt lgkmcnt(7)\n"
    "v_mfma_f32_32x32x16_bf16 v[176:191], a[0:3], a[32:35], v[144:159]\n"
    "s_waitcnt vmcnt(8) lgkmcnt(0)\n"
    "s_barrier\n"
    "ds_read_b128 a[32:35], v226 offset:10240\n"
    "ds_read_u16_d16_hi v225, v238 offset:0\n"
    "v_rcp_f32 v216, v216\n"
    "v_mfma_f32_32x32x16_bf16 v[176:191], a[4:7], a[36:39], v[176:191]\n"
    "ds_read_b128 a[36:39], v227 offset:10240\n"
    "v_add_f32 v224, v220, v221\n"
    "v_rcp_f32 v217, v217\n"
    "v_rcp_f32 v218, v218\n"
    "v_mfma_f32_32x32x16_bf16 v[176:191], a[8:11], a[40:43], v[176:191]\n"
    "ds_read_b128 a[40:43], v226 offset:10752\n"
    "v_rcp_f32 v219, v219\n"
    "v_exp_f32_e64 v160, v160 clamp\n"
    "v_mfma_f32_32x32x16_bf16 v[176:191], a[12:15], a[44:47], v[176:191]\n"
    "ds_read_b128 a[44:47], v227 offset:10752\n"
    "s_waitcnt lgkmcnt(3)\n"
    "v_mul_f32 v224, v224, v225\n"
    "v_cvt_pk_bf16_f32 v224, v224, v224\n"
    "v_exp_f32_e64 v161, v161 clamp\n"
    "v_exp_f32_e64 v162, v162 clamp\n"
    "v_mfma_f32_32x32x16_bf16 v[176:191], a[16:19], a[48:51], v[176:191]\n"
    "ds_read_b128 a[48:51], v226 offset:11264\n"
    "v_perm_b32 v247, v243, v241, s61\n"
    "v_exp_f32_e64 v163, v163 clamp\n"
    "v_exp_f32_e64 v164, v164 clamp\n"
    "v_mfma_f32_32x32x16_bf16 v[176:191], a[20:23], a[52:55], v[176:191]\n"
    "s_add_u32 m0, s80, 0x5000\n"
    "ds_read_b128 a[52:55], v227 offset:11264\n"
    "v_exp_f32_e64 v165, v165 clamp\n"
    "v_exp_f32_e64 v166, v166 clamp\n"
    "v_mfma_f32_32x32x16_bf16 v[176:191], a[24:27], a[56:59], v[176:191]\n"
    "buffer_load_dwordx4 v247, s[88:91], s48 offen offset:0 nt lds\n"
    "buffer_load_dwordx4 v247, s[88:91], s49 offen offset:1024 nt lds\n"
    "ds_read_b128 a[56:59], v226 offset:11776\n"
    "ds_write_b16 v232, v224 offset:0\n"
    "v_exp_f32_e64 v167, v167 clamp\n"
    "v_exp_f32_e64 v168, v168 clamp\n"
    "v_mfma_f32_32x32x16_bf16 v[176:191], a[28:31], a[60:63], v[176:191]\n"
    "ds_read_b128 a[60:63], v227 offset:11776\n"
    "v_exp_f32_e64 v169, v169 clamp\n"
    "v_exp_f32_e64 v170, v170 clamp\n"
    "v_exp_f32_e64 v171, v171 clamp\n"
    "v_exp_f32_e64 v172, v172 clamp\n"
    "v_exp_f32_e64 v173, v173 clamp\n"
    "v_exp_f32_e64 v174, v174 clamp\n"
    "v_exp_f32_e64 v175, v175 clamp\n"
    "v_pk_fma_f32 v[220:221], v[208:209], v[216:217], v[222:223]\n"
    "v_pk_add_f32 v[160:161], v[160:161], v[192:193]\n"
    "v_pk_add_f32 v[162:163], v[162:163], v[194:195]\n"
    "v_pk_add_f32 v[164:165], v[164:165], v[196:197]\n"
    "v_pk_add_f32 v[166:167], v[166:167], v[198:199]\n"
    "v_pk_fma_f32 v[220:221], v[212:213], v[218:219], v[220:221]\n"
    "v_pk_add_f32 v[168:169], v[168:169], v[200:201]\n"
    "v_pk_add_f32 v[170:171], v[170:171], v[202:203]\n"
    "v_pk_add_f32 v[172:173], v[172:173], v[204:205]\n"
    "v_pk_add_f32 v[174:175], v[174:175], v[206:207]\n"
    "v_pk_add_f32 v[208:209], v[160:161], v[162:163]\n"
    "v_pk_add_f32 v[210:211], v[164:165], v[166:167]\n"
    "v_pk_add_f32 v[212:213], v[168:169], v[170:171]\n"
    "v_pk_add_f32 v[214:215], v[172:173], v[174:175]\n"
    "v_pk_mul_f32 v[160:161], v[160:161], v[162:163]\n"
    "v_pk_mul_f32 v[164:165], v[164:165], v[166:167]\n"
    "v_pk_mul_f32 v[168:169], v[168:169], v[170:171]\n"
    "v_pk_mul_f32 v[172:173], v[172:173], v[174:175]\n"
    "v_pk_mul_f32 v[208:209], v[208:209], v[164:165]\n"
    "v_pk_mul_f32 v[212:213], v[212:213], v[172:173]\n"
    "v_pk_mul_f32 v[216:217], v[160:161], v[164:165]\n"
    "v_pk_mul_f32 v[218:219], v[168:169], v[172:173]\n"
    "v_pk_fma_f32 v[208:209], v[210:211], v[160:161], v[208:209]\n"
    "v_pk_fma_f32 v[212:213], v[214:215], v[168:169], v[212:213]\n"
    "v_mfma_f32_32x32x16_bf16 v[160:175], v[0:3], a[32:35], v[96:111]\n"
    "s_waitcnt vmcnt(8) lgkmcnt(0)\n"
    "s_barrier\n"
    "ds_read_b128 a[32:35], v226 offset:12288\n"
    "ds_read_u16_d16_hi v225, v238 offset:40960\n"
    "v_rcp_f32 v216, v216\n"
    "v_mfma_f32_32x32x16_bf16 v[160:175], v[4:7], a[36:39], v[160:175]\n"
    "ds_read_b128 a[36:39], v227 offset:12288\n"
    "ds_read_b128 a[64:67], v234 offset:0\n"
    "v_add_f32 v224, v220, v221\n"
    "v_rcp_f32 v217, v217\n"
    "v_rcp_f32 v218, v218\n"
    "v_mfma_f32_32x32x16_bf16 v[160:175], v[8:11], a[40:43], v[160:175]\n"
    "ds_read_b128 a[40:43], v226 offset:12800\n"
    "ds_read_b64_tr_b16 a[68:69], v228 offset:0\n"
    "v_rcp_f32 v219, v219\n"
    "v_exp_f32_e64 v176, v176 clamp\n"
    "v_mfma_f32_32x32x16_bf16 v[160:175], v[12:15], a[44:47], v[160:175]\n"
    "ds_read_b128 a[44:47], v227 offset:12800\n"
    "ds_read_b64_tr_b16 a[70:71], v229 offset:0\n"
    "s_waitcnt lgkmcnt(6)\n"
    "v_mul_f32 v224, v224, v225\n"
    "v_cvt_pk_bf16_f32 v224, v224, v224\n"
    "v_exp_f32_e64 v177, v177 clamp\n"
    "v_exp_f32_e64 v178, v178 clamp\n"
    "v_mfma_f32_32x32x16_bf16 v[160:175], v[16:19], a[48:51], v[160:175]\n"
    "ds_read_b128 a[48:51], v226 offset:13312\n"
    "ds_read_b64_tr_b16 a[72:73], v230 offset:0\n"
    "v_perm_b32 v246, v244, v241, s61\n"
    "v_exp_f32_e64 v179, v179 clamp\n"
    "v_exp_f32_e64 v180, v180 clamp\n"
    "v_mfma_f32_32x32x16_bf16 v[160:175], v[20:23], a[52:55], v[160:175]\n"
    "ds_read_b128 a[52:55], v227 offset:13312\n"
    "ds_read_b64_tr_b16 a[74:75], v231 offset:0\n"
    "v_exp_f32_e64 v181, v181 clamp\n"
    "v_exp_f32_e64 v182, v182 clamp\n"
    "v_mfma_f32_32x32x16_bf16 v[160:175], v[24:27], a[56:59], v[160:175]\n"
    "ds_read_b128 a[56:59], v226 offset:13824\n"
    "ds_write_b16 v232, v224 offset:40960\n"
    "v_exp_f32_e64 v183, v183 clamp\n"
    "v_exp_f32_e64 v184, v184 clamp\n"
    "v_mfma_f32_32x32x16_bf16 v[160:175], v[28:31], a[60:63], v[160:175]\n"
    "s_waitcnt lgkmcnt(2)\n"
    "ds_read_b128 a[60:63], v227 offset:13824\n"
    "v_exp_f32_e64 v185, v185 clamp\n"
    "v_exp_f32_e64 v186, v186 clamp\n"
    "v_exp_f32_e64 v187, v187 clamp\n"
    "v_mfma_f32_16x16x32_bf16 a[76:79], a[64:67], a[68:71], a[76:79]\n"
    "buffer_load_dwordx4 v246, s[92:95], s50 offen offset:2048 nt lds\n"
    "buffer_load_dwordx4 v246, s[92:95], s51 offen offset:3072 nt lds\n"
    "v_exp_f32_e64 v188, v188 clamp\n"
    "v_exp_f32_e64 v189, v189 clamp\n"
    "v_mfma_f32_16x16x32_bf16 a[80:83], a[64:67], a[72:75], a[80:83]\n"
    "v_exp_f32_e64 v190, v190 clamp\n"
    "v_exp_f32_e64 v191, v191 clamp\n"
    "v_pk_fma_f32 v[220:221], v[208:209], v[216:217], v[222:223]\n"
    "v_pk_add_f32 v[176:177], v[176:177], v[192:193]\n"
    "v_pk_add_f32 v[178:179], v[178:179], v[194:195]\n"
    "v_pk_add_f32 v[180:181], v[180:181], v[196:197]\n"
    "v_pk_add_f32 v[182:183], v[182:183], v[198:199]\n"
    "v_pk_fma_f32 v[220:221], v[212:213], v[218:219], v[220:221]\n"
    "v_pk_add_f32 v[184:185], v[184:185], v[200:201]\n"
    "v_pk_add_f32 v[186:187], v[186:187], v[202:203]\n"
    "v_pk_add_f32 v[188:189], v[188:189], v[204:205]\n"
    "v_pk_add_f32 v[190:191], v[190:191], v[206:207]\n"
    "v_pk_add_f32 v[208:209], v[176:177], v[178:179]\n"
    "v_pk_add_f32 v[210:211], v[180:181], v[182:183]\n"
    "v_pk_add_f32 v[212:213], v[184:185], v[186:187]\n"
    "v_pk_add_f32 v[214:215], v[188:189], v[190:191]\n"
    "v_pk_mul_f32 v[176:177], v[176:177], v[178:179]\n"
    "v_pk_mul_f32 v[180:181], v[180:181], v[182:183]\n"
    "v_pk_mul_f32 v[184:185], v[184:185], v[186:187]\n"
    "v_pk_mul_f32 v[188:189], v[188:189], v[190:191]\n"
    "v_pk_mul_f32 v[208:209], v[208:209], v[180:181]\n"
    "v_pk_mul_f32 v[212:213], v[212:213], v[188:189]\n"
    "v_pk_mul_f32 v[216:217], v[176:177], v[180:181]\n"
    "v_pk_mul_f32 v[218:219], v[184:185], v[188:189]\n"
    "v_pk_fma_f32 v[208:209], v[210:211], v[176:177], v[208:209]\n"
    "v_pk_fma_f32 v[212:213], v[214:215], v[184:185], v[212:213]\n"
    "v_mfma_f32_32x32x16_bf16 v[176:191], v[32:35], a[32:35], v[112:127]\n"
    "s_waitcnt vmcnt(8) lgkmcnt(0)\n"
    "s_barrier\n"
    "ds_read_b128 a[32:35], v226 offset:14336\n"
    "ds_read_u16_d16_hi v225, v239 offset:0\n"
    "v_rcp_f32 v216, v216\n"
    "v_mfma_f32_32x32x16_bf16 v[176:191], v[36:39], a[36:39], v[176:191]\n"
    "ds_read_b128 a[36:39], v227 offset:14336\n"
    "ds_read_b128 a[64:67], v235 offset:0\n"
    "v_add_f32 v224, v220, v221\n"
    "v_rcp_f32 v217, v217\n"
    "v_rcp_f32 v218, v218\n"
    "v_mfma_f32_32x32x16_bf16 v[176:191], v[40:43], a[40:43], v[176:191]\n"
    "ds_read_b128 a[40:43], v226 offset:14848\n"
    "ds_read_b64_tr_b16 a[68:69], v228 offset:2048\n"
    "v_rcp_f32 v219, v219\n"
    "v_exp_f32_e64 v160, v160 clamp\n"
    "v_mfma_f32_32x32x16_bf16 v[176:191], v[44:47], a[44:47], v[176:191]\n"
    "ds_read_b128 a[44:47], v227 offset:14848\n"
    "ds_read_b64_tr_b16 a[70:71], v229 offset:2048\n"
    "s_waitcnt lgkmcnt(6)\n"
    "v_mul_f32 v224, v224, v225\n"
    "v_cvt_pk_bf16_f32 v224, v224, v224\n"
    "v_exp_f32_e64 v161, v161 clamp\n"
    "v_exp_f32_e64 v162, v162 clamp\n"
    "v_mfma_f32_32x32x16_bf16 v[176:191], v[48:51], a[48:51], v[176:191]\n"
    "ds_read_b128 a[48:51], v226 offset:15360\n"
    "ds_read_b64_tr_b16 a[72:73], v230 offset:2048\n"
    "v_perm_b32 v247, v245, v241, s61\n"
    "v_exp_f32_e64 v163, v163 clamp\n"
    "v_exp_f32_e64 v164, v164 clamp\n"
    "v_mfma_f32_32x32x16_bf16 v[176:191], v[52:55], a[52:55], v[176:191]\n"
    "s_add_u32 m0, s80, 0x6000\n"
    "ds_read_b128 a[52:55], v227 offset:15360\n"
    "ds_read_b64_tr_b16 a[74:75], v231 offset:2048\n"
    "v_exp_f32_e64 v165, v165 clamp\n"
    "v_exp_f32_e64 v166, v166 clamp\n"
    "v_mfma_f32_32x32x16_bf16 v[176:191], v[56:59], a[56:59], v[176:191]\n"
    "ds_read_b128 a[56:59], v226 offset:15872\n"
    "ds_write_b16 v233, v224 offset:0\n"
    "v_exp_f32_e64 v167, v167 clamp\n"
    "v_exp_f32_e64 v168, v168 clamp\n"
    "v_mfma_f32_32x32x16_bf16 v[176:191], v[60:63], a[60:63], v[176:191]\n"
    "s_waitcnt lgkmcnt(2)\n"
    "ds_read_b128 a[60:63], v227 offset:15872\n"
    "v_exp_f32_e64 v169, v169 clamp\n"
    "v_exp_f32_e64 v170, v170 clamp\n"
    "v_exp_f32_e64 v171, v171 clamp\n"
    "v_mfma_f32_16x16x32_bf16 a[76:79], a[64:67], a[68:71], a[76:79]\n"
    "buffer_load_dwordx4 v247, s[96:99], s48 offen offset:0 nt lds\n"
    "buffer_load_dwordx4 v247, s[96:99], s49 offen offset:1024 nt lds\n"
    "v_exp_f32_e64 v172, v172 clamp\n"
    "v_exp_f32_e64 v173, v173 clamp\n"
    "v_mfma_f32_16x16x32_bf16 a[80:83], a[64:67], a[72:75], a[80:83]\n"
    "v_exp_f32_e64 v174, v174 clamp\n"
    "v_exp_f32_e64 v175, v175 clamp\n"
    "v_pk_fma_f32 v[220:221], v[208:209], v[216:217], v[222:223]\n"
    "v_pk_add_f32 v[160:161], v[160:161], v[192:193]\n"
    "v_pk_add_f32 v[162:163], v[162:163], v[194:195]\n"
    "v_pk_add_f32 v[164:165], v[164:165], v[196:197]\n"
    "v_pk_add_f32 v[166:167], v[166:167], v[198:199]\n"
    "v_pk_fma_f32 v[220:221], v[212:213], v[218:219], v[220:221]\n"
    "v_pk_add_f32 v[168:169], v[168:169], v[200:201]\n"
    "v_pk_add_f32 v[170:171], v[170:171], v[202:203]\n"
    "v_pk_add_f32 v[172:173], v[172:173], v[204:205]\n"
    "v_pk_add_f32 v[174:175], v[174:175], v[206:207]\n"
    "v_pk_add_f32 v[208:209], v[160:161], v[162:163]\n"
    "v_pk_add_f32 v[210:211], v[164:165], v[166:167]\n"
    "v_pk_add_f32 v[212:213], v[168:169], v[170:171]\n"
    "v_pk_add_f32 v[214:215], v[172:173], v[174:175]\n"
    "v_pk_mul_f32 v[160:161], v[160:161], v[162:163]\n"
    "v_pk_mul_f32 v[164:165], v[164:165], v[166:167]\n"
    "v_pk_mul_f32 v[168:169], v[168:169], v[170:171]\n"
    "v_pk_mul_f32 v[172:173], v[172:173], v[174:175]\n"
    "v_pk_mul_f32 v[208:209], v[208:209], v[164:165]\n"
    "v_pk_mul_f32 v[212:213], v[212:213], v[172:173]\n"
    "v_pk_mul_f32 v[216:217], v[160:161], v[164:165]\n"
    "v_pk_mul_f32 v[218:219], v[168:169], v[172:173]\n"
    "v_pk_fma_f32 v[208:209], v[210:211], v[160:161], v[208:209]\n"
    "v_pk_fma_f32 v[212:213], v[214:215], v[168:169], v[212:213]\n"
    "v_mfma_f32_32x32x16_bf16 v[160:175], v[64:67], a[32:35], v[128:143]\n"
    "s_waitcnt vmcnt(8) lgkmcnt(0)\n"
    "s_barrier\n"
    "ds_read_b128 a[32:35], v226 offset:16384\n"
    "ds_read_u16_d16_hi v225, v239 offset:40960\n"
    "v_rcp_f32 v216, v216\n"
    "v_mfma_f32_32x32x16_bf16 v[160:175], v[68:71], a[36:39], v[160:175]\n"
    "ds_read_b128 a[36:39], v227 offset:16384\n"
    "ds_read_b128 a[64:67], v236 offset:0\n"
    "v_add_f32 v224, v220, v221\n"
    "v_rcp_f32 v217, v217\n"
    "v_rcp_f32 v218, v218\n"
    "v_mfma_f32_32x32x16_bf16 v[160:175], v[72:75], a[40:43], v[160:175]\n"
    "ds_read_b128 a[40:43], v226 offset:16896\n"
    "ds_read_b64_tr_b16 a[68:69], v228 offset:4096\n"
    "v_rcp_f32 v219, v219\n"
    "v_exp_f32_e64 v176, v176 clamp\n"
    "v_mfma_f32_32x32x16_bf16 v[160:175], v[76:79], a[44:47], v[160:175]\n"
    "ds_read_b128 a[44:47], v227 offset:16896\n"
    "ds_read_b64_tr_b16 a[70:71], v229 offset:4096\n"
    "s_waitcnt lgkmcnt(6)\n"
    "v_mul_f32 v224, v224, v225\n"
    "v_cvt_pk_bf16_f32 v224, v224, v224\n"
    "v_exp_f32_e64 v177, v177 clamp\n"
    "v_exp_f32_e64 v178, v178 clamp\n"
    "v_mfma_f32_32x32x16_bf16 v[160:175], v[80:83], a[48:51], v[160:175]\n"
    "ds_read_b128 a[48:51], v226 offset:17408\n"
    "ds_read_b64_tr_b16 a[72:73], v230 offset:4096\n"
    "v_perm_b32 v246, v242, v241, s62\n"
    "v_exp_f32_e64 v179, v179 clamp\n"
    "v_exp_f32_e64 v180, v180 clamp\n"
    "v_mfma_f32_32x32x16_bf16 v[160:175], v[84:87], a[52:55], v[160:175]\n"
    "ds_read_b128 a[52:55], v227 offset:17408\n"
    "ds_read_b64_tr_b16 a[74:75], v231 offset:4096\n"
    "v_exp_f32_e64 v181, v181 clamp\n"
    "v_exp_f32_e64 v182, v182 clamp\n"
    "v_mfma_f32_32x32x16_bf16 v[160:175], v[88:91], a[56:59], v[160:175]\n"
    "ds_read_b128 a[56:59], v226 offset:17920\n"
    "ds_write_b16 v233, v224 offset:40960\n"
    "v_exp_f32_e64 v183, v183 clamp\n"
    "v_exp_f32_e64 v184, v184 clamp\n"
    "v_mfma_f32_32x32x16_bf16 v[160:175], v[92:95], a[60:63], v[160:175]\n"
    "s_waitcnt lgkmcnt(2)\n"
    "ds_read_b128 a[60:63], v227 offset:17920\n"
    "v_exp_f32_e64 v185, v185 clamp\n"
    "v_exp_f32_e64 v186, v186 clamp\n"
    "v_exp_f32_e64 v187, v187 clamp\n"
    "v_mfma_f32_16x16x32_bf16 a[84:87], a[64:67], a[68:71], a[84:87]\n"
    "buffer_load_dwordx4 v246, s[84:87], s50 offen offset:2048 nt lds\n"
    "buffer_load_dwordx4 v246, s[84:87], s51 offen offset:3072 nt lds\n"
    "v_exp_f32_e64 v188, v188 clamp\n"
    "v_exp_f32_e64 v189, v189 clamp\n"
    "v_mfma_f32_16x16x32_bf16 a[88:91], a[64:67], a[72:75], a[88:91]\n"
    "v_exp_f32_e64 v190, v190 clamp\n"
    "v_exp_f32_e64 v191, v191 clamp\n"
    "v_pk_fma_f32 v[220:221], v[208:209], v[216:217], v[222:223]\n"
    "v_pk_add_f32 v[176:177], v[176:177], v[192:193]\n"
    "v_pk_add_f32 v[178:179], v[178:179], v[194:195]\n"
    "v_pk_add_f32 v[180:181], v[180:181], v[196:197]\n"
    "v_pk_add_f32 v[182:183], v[182:183], v[198:199]\n"
    "v_pk_fma_f32 v[220:221], v[212:213], v[218:219], v[220:221]\n"
    "v_pk_add_f32 v[184:185], v[184:185], v[200:201]\n"
    "v_pk_add_f32 v[186:187], v[186:187], v[202:203]\n"
    "v_pk_add_f32 v[188:189], v[188:189], v[204:205]\n"
    "v_pk_add_f32 v[190:191], v[190:191], v[206:207]\n"
    "v_pk_add_f32 v[208:209], v[176:177], v[178:179]\n"
    "v_pk_add_f32 v[210:211], v[180:181], v[182:183]\n"
    "v_pk_add_f32 v[212:213], v[184:185], v[186:187]\n"
    "v_pk_add_f32 v[214:215], v[188:189], v[190:191]\n"
    "v_pk_mul_f32 v[176:177], v[176:177], v[178:179]\n"
    "v_pk_mul_f32 v[180:181], v[180:181], v[182:183]\n"
    "v_pk_mul_f32 v[184:185], v[184:185], v[186:187]\n"
    "v_pk_mul_f32 v[188:189], v[188:189], v[190:191]\n"
    "v_pk_mul_f32 v[208:209], v[208:209], v[180:181]\n"
    "v_pk_mul_f32 v[212:213], v[212:213], v[188:189]\n"
    "v_pk_mul_f32 v[216:217], v[176:177], v[180:181]\n"
    "v_pk_mul_f32 v[218:219], v[184:185], v[188:189]\n"
    "v_pk_fma_f32 v[208:209], v[210:211], v[176:177], v[208:209]\n"
    "v_pk_fma_f32 v[212:213], v[214:215], v[184:185], v[212:213]\n"
    "v_mfma_f32_32x32x16_bf16 v[176:191], a[0:3], a[32:35], v[144:159]\n"
    "s_waitcnt vmcnt(8) lgkmcnt(0)\n"
    "s_barrier\n"
    "ds_read_b128 a[32:35], v226 offset:18432\n"
    "ds_read_u16_d16_hi v225, v238 offset:192\n"
    "v_rcp_f32 v216, v216\n"
    "v_mfma_f32_32x32x16_bf16 v[176:191], a[4:7], a[36:39], v[176:191]\n"
    "ds_read_b128 a[36:39], v227 offset:18432\n"
    "ds_read_b128 a[64:67], v237 offset:0\n"
    "v_add_f32 v224, v220, v221\n"
    "v_rcp_f32 v217, v217\n"
    "v_rcp_f32 v218, v218\n"
    "v_mfma_f32_32x32x16_bf16 v[176:191], a[8:11], a[40:43], v[176:191]\n"
    "ds_read_b128 a[40:43], v226 offset:18944\n"
    "ds_read_b64_tr_b16 a[68:69], v228 offset:8192\n"
    "v_rcp_f32 v219, v219\n"
    "v_exp_f32_e64 v160, v160 clamp\n"
    "v_mfma_f32_32x32x16_bf16 v[176:191], a[12:15], a[44:47], v[176:191]\n"
    "ds_read_b128 a[44:47], v227 offset:18944\n"
    "ds_read_b64_tr_b16 a[70:71], v229 offset:8192\n"
    "s_waitcnt lgkmcnt(6)\n"
    "v_mul_f32 v224, v224, v225\n"
    "v_cvt_pk_bf16_f32 v224, v224, v224\n"
    "v_exp_f32_e64 v161, v161 clamp\n"
    "v_exp_f32_e64 v162, v162 clamp\n"
    "v_mfma_f32_32x32x16_bf16 v[176:191], a[16:19], a[48:51], v[176:191]\n"
    "ds_read_b128 a[48:51], v226 offset:19456\n"
    "ds_read_b64_tr_b16 a[72:73], v230 offset:8192\n"
    "v_perm_b32 v247, v243, v241, s62\n"
    "v_exp_f32_e64 v163, v163 clamp\n"
    "v_exp_f32_e64 v164, v164 clamp\n"
    "v_mfma_f32_32x32x16_bf16 v[176:191], a[20:23], a[52:55], v[176:191]\n"
    "s_add_u32 m0, s80, 0x7000\n"
    "ds_read_b128 a[52:55], v227 offset:19456\n"
    "ds_read_b64_tr_b16 a[74:75], v231 offset:8192\n"
    "v_exp_f32_e64 v165, v165 clamp\n"
    "v_exp_f32_e64 v166, v166 clamp\n"
    "v_mfma_f32_32x32x16_bf16 v[176:191], a[24:27], a[56:59], v[176:191]\n"
    "ds_read_b128 a[56:59], v226 offset:19968\n"
    "ds_write_b16 v232, v224 offset:64\n"
    "v_exp_f32_e64 v167, v167 clamp\n"
    "v_exp_f32_e64 v168, v168 clamp\n"
    "v_mfma_f32_32x32x16_bf16 v[176:191], a[28:31], a[60:63], v[176:191]\n"
    "s_waitcnt lgkmcnt(2)\n"
    "ds_read_b128 a[60:63], v227 offset:19968\n"
    "v_exp_f32_e64 v169, v169 clamp\n"
    "v_exp_f32_e64 v170, v170 clamp\n"
    "v_exp_f32_e64 v171, v171 clamp\n"
    "v_mfma_f32_16x16x32_bf16 a[84:87], a[64:67], a[68:71], a[84:87]\n"
    "buffer_load_dwordx4 v247, s[88:91], s48 offen offset:0 nt lds\n"
    "buffer_load_dwordx4 v247, s[88:91], s49 offen offset:1024 nt lds\n"
    "v_exp_f32_e64 v172, v172 clamp\n"
    "v_exp_f32_e64 v173, v173 clamp\n"
    "v_mfma_f32_16x16x32_bf16 a[88:91], a[64:67], a[72:75], a[88:91]\n"
    "v_exp_f32_e64 v174, v174 clamp\n"
    "v_exp_f32_e64 v175, v175 clamp\n"
    "v_pk_fma_f32 v[220:221], v[208:209], v[216:217], v[222:223]\n"
    "v_pk_add_f32 v[160:161], v[160:161], v[192:193]\n"
    "v_pk_add_f32 v[162:163], v[162:163], v[194:195]\n"
    "v_pk_add_f32 v[164:165], v[164:165], v[196:197]\n"
    "v_pk_add_f32 v[166:167], v[166:167], v[198:199]\n"
    "v_pk_fma_f32 v[220:221], v[212:213], v[218:219], v[220:221]\n"
    "v_pk_add_f32 v[168:169], v[168:169], v[200:201]\n"
    "v_pk_add_f32 v[170:171], v[170:171], v[202:203]\n"
    "v_pk_add_f32 v[172:173], v[172:173], v[204:205]\n"
    "v_pk_add_f32 v[174:175], v[174:175], v[206:207]\n"
    "v_pk_add_f32 v[208:209], v[160:161], v[162:163]\n"
    "v_pk_add_f32 v[210:211], v[164:165], v[166:167]\n"
    "v_pk_add_f32 v[212:213], v[168:169], v[170:171]\n"
    "v_pk_add_f32 v[214:215], v[172:173], v[174:175]\n"
    "v_pk_mul_f32 v[160:161], v[160:161], v[162:163]\n"
    "v_pk_mul_f32 v[164:165], v[164:165], v[166:167]\n"
    "v_pk_mul_f32 v[168:169], v[168:169], v[170:171]\n"
    "v_pk_mul_f32 v[172:173], v[172:173], v[174:175]\n"
    "v_pk_mul_f32 v[208:209], v[208:209], v[164:165]\n"
    "v_pk_mul_f32 v[212:213], v[212:213], v[172:173]\n"
    "v_pk_mul_f32 v[216:217], v[160:161], v[164:165]\n"
    "v_pk_mul_f32 v[218:219], v[168:169], v[172:173]\n"
    "v_pk_fma_f32 v[208:209], v[210:211], v[160:161], v[208:209]\n"
    "v_pk_fma_f32 v[212:213], v[214:215], v[168:169], v[212:213]\n"
    "v_mfma_f32_32x32x16_bf16 v[160:175], v[0:3], a[32:35], v[96:111]\n"
    "s_waitcnt vmcnt(8) lgkmcnt(0)\n"
    "s_barrier\n"
    "ds_read_b128 a[32:35], v226 offset:20480\n"
    "ds_read_u16_d16_hi v225, v238 offset:41152\n"
    "v_rcp_f32 v216, v216\n"
    "v_mfma_f32_32x32x16_bf16 v[160:175], v[4:7], a[36:39], v[160:175]\n"
    "ds_read_b128 a[36:39], v227 offset:20480\n"
    "ds_read_b128 a[64:67], v234 offset:64\n"
    "v_add_f32 v224, v220, v221\n"
    "v_rcp_f32 v217, v217\n"
    "v_rcp_f32 v218, v218\n"
    "v_mfma_f32_32x32x16_bf16 v[160:175], v[8:11], a[40:43], v[160:175]\n"
    "ds_read_b128 a[40:43], v226 offset:20992\n"
    "ds_read_b64_tr_b16 a[68:69], v228 offset:10240\n"
    "v_rcp_f32 v219, v219\n"
    "v_exp_f32_e64 v176, v176 clamp\n"
    "v_mfma_f32_32x32x16_bf16 v[160:175], v[12:15], a[44:47], v[160:175]\n"
    "ds_read_b128 a[44:47], v227 offset:20992\n"
    "ds_read_b64_tr_b16 a[70:71], v229 offset:10240\n"
    "s_waitcnt lgkmcnt(6)\n"
    "v_mul_f32 v224, v224, v225\n"
    "v_cvt_pk_bf16_f32 v224, v224, v224\n"
    "v_exp_f32_e64 v177, v177 clamp\n"
    "v_exp_f32_e64 v178, v178 clamp\n"
    "v_mfma_f32_32x32x16_bf16 v[160:175], v[16:19], a[48:51], v[160:175]\n"
    "ds_read_b128 a[48:51], v226 offset:21504\n"
    "ds_read_b64_tr_b16 a[72:73], v230 offset:10240\n"
    "v_perm_b32 v246, v244, v241, s62\n"
    "v_exp_f32_e64 v179, v179 clamp\n"
    "v_exp_f32_e64 v180, v180 clamp\n"
    "v_mfma_f32_32x32x16_bf16 v[160:175], v[20:23], a[52:55], v[160:175]\n"
    "ds_read_b128 a[52:55], v227 offset:21504\n"
    "ds_read_b64_tr_b16 a[74:75], v231 offset:10240\n"
    "v_exp_f32_e64 v181, v181 clamp\n"
    "v_exp_f32_e64 v182, v182 clamp\n"
    "v_mfma_f32_32x32x16_bf16 v[160:175], v[24:27], a[56:59], v[160:175]\n"
    "ds_read_b128 a[56:59], v226 offset:22016\n"
    "ds_write_b16 v232, v224 offset:41024\n"
    "v_exp_f32_e64 v183, v183 clamp\n"
    "v_exp_f32_e64 v184, v184 clamp\n"
    "v_mfma_f32_32x32x16_bf16 v[160:175], v[28:31], a[60:63], v[160:175]\n"
    "s_waitcnt lgkmcnt(2)\n"
    "ds_read_b128 a[60:63], v227 offset:22016\n"
    "v_exp_f32_e64 v185, v185 clamp\n"
    "v_exp_f32_e64 v186, v186 clamp\n"
    "v_exp_f32_e64 v187, v187 clamp\n"
    "v_mfma_f32_16x16x32_bf16 a[76:79], a[64:67], a[68:71], a[76:79]\n"
    "buffer_load_dwordx4 v246, s[92:95], s50 offen offset:2048 nt lds\n"
    "buffer_load_dwordx4 v246, s[92:95], s51 offen offset:3072 nt lds\n"
    "v_exp_f32_e64 v188, v188 clamp\n"
    "v_exp_f32_e64 v189, v189 clamp\n"
    "v_mfma_f32_16x16x32_bf16 a[80:83], a[64:67], a[72:75], a[80:83]\n"
    "v_exp_f32_e64 v190, v190 clamp\n"
    "v_exp_f32_e64 v191, v191 clamp\n"
    "v_pk_fma_f32 v[220:221], v[208:209], v[216:217], v[222:223]\n"
    "v_pk_add_f32 v[176:177], v[176:177], v[192:193]\n"
    "v_pk_add_f32 v[178:179], v[178:179], v[194:195]\n"
    "v_pk_add_f32 v[180:181], v[180:181], v[196:197]\n"
    "v_pk_add_f32 v[182:183], v[182:183], v[198:199]\n"
    "v_pk_fma_f32 v[220:221], v[212:213], v[218:219], v[220:221]\n"
    "v_pk_add_f32 v[184:185], v[184:185], v[200:201]\n"
    "v_pk_add_f32 v[186:187], v[186:187], v[202:203]\n"
    "v_pk_add_f32 v[188:189], v[188:189], v[204:205]\n"
    "v_pk_add_f32 v[190:191], v[190:191], v[206:207]\n"
    "v_pk_add_f32 v[208:209], v[176:177], v[178:179]\n"
    "v_pk_add_f32 v[210:211], v[180:181], v[182:183]\n"
    "v_pk_add_f32 v[212:213], v[184:185], v[186:187]\n"
    "v_pk_add_f32 v[214:215], v[188:189], v[190:191]\n"
    "v_pk_mul_f32 v[176:177], v[176:177], v[178:179]\n"
    "v_pk_mul_f32 v[180:181], v[180:181], v[182:183]\n"
    "v_pk_mul_f32 v[184:185], v[184:185], v[186:187]\n"
    "v_pk_mul_f32 v[188:189], v[188:189], v[190:191]\n"
    "v_pk_mul_f32 v[208:209], v[208:209], v[180:181]\n"
    "v_pk_mul_f32 v[212:213], v[212:213], v[188:189]\n"
    "v_pk_mul_f32 v[216:217], v[176:177], v[180:181]\n"
    "v_pk_mul_f32 v[218:219], v[184:185], v[188:189]\n"
    "v_pk_fma_f32 v[208:209], v[210:211], v[176:177], v[208:209]\n"
    "v_pk_fma_f32 v[212:213], v[214:215], v[184:185], v[212:213]\n"
    "v_mfma_f32_32x32x16_bf16 v[176:191], v[32:35], a[32:35], v[112:127]\n"
    "s_waitcnt vmcnt(8) lgkmcnt(0)\n"
    "s_barrier\n"
    "ds_read_b128 a[32:35], v226 offset:22528\n"
    "ds_read_u16_d16_hi v225, v239 offset:192\n"
    "v_rcp_f32 v216, v216\n"
    "v_mfma_f32_32x32x16_bf16 v[176:191], v[36:39], a[36:39], v[176:191]\n"
    "ds_read_b128 a[36:39], v227 offset:22528\n"
    "ds_read_b128 a[64:67], v235 offset:64\n"
    "v_add_f32 v224, v220, v221\n"
    "v_rcp_f32 v217, v217\n"
    "v_rcp_f32 v218, v218\n"
    "v_mfma_f32_32x32x16_bf16 v[176:191], v[40:43], a[40:43], v[176:191]\n"
    "ds_read_b128 a[40:43], v226 offset:23040\n"
    "ds_read_b64_tr_b16 a[68:69], v228 offset:12288\n"
    "v_rcp_f32 v219, v219\n"
    "v_exp_f32_e64 v160, v160 clamp\n"
    "v_mfma_f32_32x32x16_bf16 v[176:191], v[44:47], a[44:47], v[176:191]\n"
    "ds_read_b128 a[44:47], v227 offset:23040\n"
    "ds_read_b64_tr_b16 a[70:71], v229 offset:12288\n"
    "s_waitcnt lgkmcnt(6)\n"
    "v_mul_f32 v224, v224, v225\n"
    "v_cvt_pk_bf16_f32 v224, v224, v224\n"
    "v_exp_f32_e64 v161, v161 clamp\n"
    "v_exp_f32_e64 v162, v162 clamp\n"
    "v_mfma_f32_32x32x16_bf16 v[176:191], v[48:51], a[48:51], v[176:191]\n"
    "ds_read_b128 a[48:51], v226 offset:23552\n"
    "ds_read_b64_tr_b16 a[72:73], v230 offset:12288\n"
    "v_perm_b32 v247, v245, v241, s62\n"
    "v_exp_f32_e64 v163, v163 clamp\n"
    "v_exp_f32_e64 v164, v164 clamp\n"
    "v_mfma_f32_32x32x16_bf16 v[176:191], v[52:55], a[52:55], v[176:191]\n"
    "s_add_u32 m0, s80, 0x1000\n"
    "ds_read_b128 a[52:55], v227 offset:23552\n"
    "ds_read_b64_tr_b16 a[74:75], v231 offset:12288\n"
    "v_exp_f32_e64 v165, v165 clamp\n"
    "v_exp_f32_e64 v166, v166 clamp\n"
    "v_mfma_f32_32x32x16_bf16 v[176:191], v[56:59], a[56:59], v[176:191]\n"
    "ds_read_b128 a[56:59], v226 offset:24064\n"
    "ds_write_b16 v233, v224 offset:64\n"
    "v_exp_f32_e64 v167, v167 clamp\n"
    "v_exp_f32_e64 v168, v168 clamp\n"
    "v_mfma_f32_32x32x16_bf16 v[176:191], v[60:63], a[60:63], v[176:191]\n"
    "s_waitcnt lgkmcnt(2)\n"
    "ds_read_b128 a[60:63], v227 offset:24064\n"
    "v_exp_f32_e64 v169, v169 clamp\n"
    "v_exp_f32_e64 v170, v170 clamp\n"
    "v_exp_f32_e64 v171, v171 clamp\n"
    "v_mfma_f32_16x16x32_bf16 a[76:79], a[64:67], a[68:71], a[76:79]\n"
    "buffer_load_dwordx4 v247, s[96:99], s50 offen offset:2048 nt lds\n"
    "buffer_load_dwordx4 v247, s[96:99], s51 offen offset:3072 nt lds\n"
    "v_exp_f32_e64 v172, v172 clamp\n"
    "v_exp_f32_e64 v173, v173 clamp\n"
    "v_mfma_f32_16x16x32_bf16 a[80:83], a[64:67], a[72:75], a[80:83]\n"
    "v_exp_f32_e64 v174, v174 clamp\n"
    "v_exp_f32_e64 v175, v175 clamp\n"
    "v_pk_fma_f32 v[220:221], v[208:209], v[216:217], v[222:223]\n"
    "v_pk_add_f32 v[160:161], v[160:161], v[192:193]\n"
    "v_pk_add_f32 v[162:163], v[162:163], v[194:195]\n"
    "v_pk_add_f32 v[164:165], v[164:165], v[196:197]\n"
    "v_pk_add_f32 v[166:167], v[166:167], v[198:199]\n"
    "v_pk_fma_f32 v[220:221], v[212:213], v[218:219], v[220:221]\n"
    "v_pk_add_f32 v[168:169], v[168:169], v[200:201]\n"
    "v_pk_add_f32 v[170:171], v[170:171], v[202:203]\n"
    "v_pk_add_f32 v[172:173], v[172:173], v[204:205]\n"
    "v_pk_add_f32 v[174:175], v[174:175], v[206:207]\n"
    "v_pk_add_f32 v[208:209], v[160:161], v[162:163]\n"
    "v_pk_add_f32 v[210:211], v[164:165], v[166:167]\n"
    "v_pk_add_f32 v[212:213], v[168:169], v[170:171]\n"
    "v_pk_add_f32 v[214:215], v[172:173], v[174:175]\n"
    "v_pk_mul_f32 v[160:161], v[160:161], v[162:163]\n"
    "v_pk_mul_f32 v[164:165], v[164:165], v[166:167]\n"
    "v_pk_mul_f32 v[168:169], v[168:169], v[170:171]\n"
    "v_pk_mul_f32 v[172:173], v[172:173], v[174:175]\n"
    "v_pk_mul_f32 v[208:209], v[208:209], v[164:165]\n"
    "v_pk_mul_f32 v[212:213], v[212:213], v[172:173]\n"
    "v_pk_mul_f32 v[216:217], v[160:161], v[164:165]\n"
    "v_pk_mul_f32 v[218:219], v[168:169], v[172:173]\n"
    "v_pk_fma_f32 v[208:209], v[210:211], v[160:161], v[208:209]\n"
    "v_pk_fma_f32 v[212:213], v[214:215], v[168:169], v[212:213]\n"
    "v_mfma_f32_32x32x16_bf16 v[160:175], v[64:67], a[32:35], v[128:143]\n"
    "s_waitcnt vmcnt(8) lgkmcnt(0)\n"
    "s_barrier\n"
    "ds_read_b128 a[32:35], v226 offset:24576\n"
    "ds_read_u16_d16_hi v225, v239 offset:41152\n"
    "v_rcp_f32 v216, v216\n"
    "v_mfma_f32_32x32x16_bf16 v[160:175], v[68:71], a[36:39], v[160:175]\n"
    "ds_read_b128 a[36:39], v227 offset:24576\n"
    "ds_read_b128 a[64:67], v236 offset:64\n"
    "v_add_f32 v224, v220, v221\n"
    "v_rcp_f32 v217, v217\n"
    "v_rcp_f32 v218, v218\n"
    "v_mfma_f32_32x32x16_bf16 v[160:175], v[72:75], a[40:43], v[160:175]\n"
    "ds_read_b128 a[40:43], v226 offset:25088\n"
    "ds_read_b64_tr_b16 a[68:69], v228 offset:14336\n"
    "v_rcp_f32 v219, v219\n"
    "v_exp_f32_e64 v176, v176 clamp\n"
    "v_mfma_f32_32x32x16_bf16 v[160:175], v[76:79], a[44:47], v[160:175]\n"
    "ds_read_b128 a[44:47], v227 offset:25088\n"
    "ds_read_b64_tr_b16 a[70:71], v229 offset:14336\n"
    "s_waitcnt lgkmcnt(6)\n"
    "v_mul_f32 v224, v224, v225\n"
    "v_cvt_pk_bf16_f32 v224, v224, v224\n"
    "v_exp_f32_e64 v177, v177 clamp\n"
    "v_exp_f32_e64 v178, v178 clamp\n"
    "v_mfma_f32_32x32x16_bf16 v[160:175], v[80:83], a[48:51], v[160:175]\n"
    "ds_read_b128 a[48:51], v226 offset:25600\n"
    "ds_read_b64_tr_b16 a[72:73], v230 offset:14336\n"
    "v_exp_f32_e64 v179, v179 clamp\n"
    "v_exp_f32_e64 v180, v180 clamp\n"
    "v_mfma_f32_32x32x16_bf16 v[160:175], v[84:87], a[52:55], v[160:175]\n"
    "ds_read_b128 a[52:55], v227 offset:25600\n"
    "ds_read_b64_tr_b16 a[74:75], v231 offset:14336\n"
    "v_exp_f32_e64 v181, v181 clamp\n"
    "v_exp_f32_e64 v182, v182 clamp\n"
    "v_mfma_f32_32x32x16_bf16 v[160:175], v[88:91], a[56:59], v[160:175]\n"
    "ds_read_b128 a[56:59], v226 offset:26112\n"
    "ds_write_b16 v233, v224 offset:41024\n"
    "v_exp_f32_e64 v183, v183 clamp\n"
    "v_exp_f32_e64 v184, v184 clamp\n"
    "v_mfma_f32_32x32x16_bf16 v[160:175], v[92:95], a[60:63], v[160:175]\n"
    "s_waitcnt lgkmcnt(2)\n"
    "ds_read_b128 a[60:63], v227 offset:26112\n"
    "v_exp_f32_e64 v185, v185 clamp\n"
    "v_exp_f32_e64 v186, v186 clamp\n"
    "v_exp_f32_e64 v187, v187 clamp\n"
    "v_mfma_f32_16x16x32_bf16 a[84:87], a[64:67], a[68:71], a[84:87]\n"
    "v_exp_f32_e64 v188, v188 clamp\n"
    "v_exp_f32_e64 v189, v189 clamp\n"
    "v_mfma_f32_16x16x32_bf16 a[88:91], a[64:67], a[72:75], a[88:91]\n"
    "v_exp_f32_e64 v190, v190 clamp\n"
    "v_exp_f32_e64 v191, v191 clamp\n"
    "v_pk_fma_f32 v[220:221], v[208:209], v[216:217], v[222:223]\n"
    "v_pk_add_f32 v[176:177], v[176:177], v[192:193]\n"
    "v_pk_add_f32 v[178:179], v[178:179], v[194:195]\n"
    "v_pk_add_f32 v[180:181], v[180:181], v[196:197]\n"
    "v_pk_add_f32 v[182:183], v[182:183], v[198:199]\n"
    "v_pk_fma_f32 v[220:221], v[212:213], v[218:219], v[220:221]\n"
    "v_pk_add_f32 v[184:185], v[184:185], v[200:201]\n"
    "v_pk_add_f32 v[186:187], v[186:187], v[202:203]\n"
    "v_pk_add_f32 v[188:189], v[188:189], v[204:205]\n"
    "v_pk_add_f32 v[190:191], v[190:191], v[206:207]\n"
    "v_pk_add_f32 v[208:209], v[176:177], v[178:179]\n"
    "v_pk_add_f32 v[210:211], v[180:181], v[182:183]\n"
    "v_pk_add_f32 v[212:213], v[184:185], v[186:187]\n"
    "v_pk_add_f32 v[214:215], v[188:189], v[190:191]\n"
    "v_pk_mul_f32 v[176:177], v[176:177], v[178:179]\n"
    "v_pk_mul_f32 v[180:181], v[180:181], v[182:183]\n"
    "v_pk_mul_f32 v[184:185], v[184:185], v[186:187]\n"
    "v_pk_mul_f32 v[188:189], v[188:189], v[190:191]\n"
    "v_pk_mul_f32 v[208:209], v[208:209], v[180:181]\n"
    "v_pk_mul_f32 v[212:213], v[212:213], v[188:189]\n"
    "v_pk_mul_f32 v[216:217], v[176:177], v[180:181]\n"
    "v_pk_mul_f32 v[218:219], v[184:185], v[188:189]\n"
    "v_pk_fma_f32 v[208:209], v[210:211], v[176:177], v[208:209]\n"
    "v_pk_fma_f32 v[212:213], v[214:215], v[184:185], v[212:213]\n"
    "v_mfma_f32_32x32x16_bf16 v[176:191], a[0:3], a[32:35], v[144:159]\n"
    "s_waitcnt vmcnt(6) lgkmcnt(0)\n"
    "s_barrier\n"
    "ds_read_b128 a[32:35], v226 offset:26624\n"
    "ds_read_u16_d16_hi v225, v238 offset:64\n"
    "v_rcp_f32 v216, v216\n"
    "v_mfma_f32_32x32x16_bf16 v[176:191], a[4:7], a[36:39], v[176:191]\n"
    "ds_read_b128 a[36:39], v227 offset:26624\n"
    "ds_read_b128 a[64:67], v237 offset:64\n"
    "v_add_f32 v224, v220, v221\n"
    "v_rcp_f32 v217, v217\n"
    "v_rcp_f32 v218, v218\n"
    "v_mfma_f32_32x32x16_bf16 v[176:191], a[8:11], a[40:43], v[176:191]\n"
    "ds_read_b128 a[40:43], v226 offset:27136\n"
    "ds_read_b64_tr_b16 a[68:69], v228 offset:16384\n"
    "v_rcp_f32 v219, v219\n"
    "v_exp_f32_e64 v160, v160 clamp\n"
    "v_mfma_f32_32x32x16_bf16 v[176:191], a[12:15], a[44:47], v[176:191]\n"
    "ds_read_b128 a[44:47], v227 offset:27136\n"
    "ds_read_b64_tr_b16 a[70:71], v229 offset:16384\n"
    "s_waitcnt lgkmcnt(6)\n"
    "v_mul_f32 v224, v224, v225\n"
    "v_cvt_pk_bf16_f32 v224, v224, v224\n"
    "v_exp_f32_e64 v161, v161 clamp\n"
    "v_exp_f32_e64 v162, v162 clamp\n"
    "v_mfma_f32_32x32x16_bf16 v[176:191], a[16:19], a[48:51], v[176:191]\n"
    "ds_read_b128 a[48:51], v226 offset:27648\n"
    "ds_read_b64_tr_b16 a[72:73], v230 offset:16384\n"
    "v_exp_f32_e64 v163, v163 clamp\n"
    "v_exp_f32_e64 v164, v164 clamp\n"
    "v_mfma_f32_32x32x16_bf16 v[176:191], a[20:23], a[52:55], v[176:191]\n"
    "ds_read_b128 a[52:55], v227 offset:27648\n"
    "ds_read_b64_tr_b16 a[74:75], v231 offset:16384\n"
    "v_exp_f32_e64 v165, v165 clamp\n"
    "v_exp_f32_e64 v166, v166 clamp\n"
    "v_mfma_f32_32x32x16_bf16 v[176:191], a[24:27], a[56:59], v[176:191]\n"
    "ds_read_b128 a[56:59], v226 offset:28160\n"
    "ds_write_b16 v232, v224 offset:128\n"
    "v_exp_f32_e64 v167, v167 clamp\n"
    "v_exp_f32_e64 v168, v168 clamp\n"
    "v_mfma_f32_32x32x16_bf16 v[176:191], a[28:31], a[60:63], v[176:191]\n"
    "s_waitcnt lgkmcnt(2)\n"
    "ds_read_b128 a[60:63], v227 offset:28160\n"
    "v_exp_f32_e64 v169, v169 clamp\n"
    "v_exp_f32_e64 v170, v170 clamp\n"
    "v_exp_f32_e64 v171, v171 clamp\n"
    "v_mfma_f32_16x16x32_bf16 a[84:87], a[64:67], a[68:71], a[84:87]\n"
    "v_exp_f32_e64 v172, v172 clamp\n"
    "v_exp_f32_e64 v173, v173 clamp\n"
    "v_mfma_f32_16x16x32_bf16 a[88:91], a[64:67], a[72:75], a[88:91]\n"
    "v_exp_f32_e64 v174, v174 clamp\n"
    "v_exp_f32_e64 v175, v175 clamp\n"
    "v_pk_fma_f32 v[220:221], v[208:209], v[216:217], v[222:223]\n"
    "v_pk_add_f32 v[160:161], v[160:161], v[192:193]\n"
    "v_pk_add_f32 v[162:163], v[162:163], v[194:195]\n"
    "v_pk_add_f32 v[164:165], v[164:165], v[196:197]\n"
    "v_pk_add_f32 v[166:167], v[166:167], v[198:199]\n"
    "v_pk_fma_f32 v[220:221], v[212:213], v[218:219], v[220:221]\n"
    "v_pk_add_f32 v[168:169], v[168:169], v[200:201]\n"
    "v_pk_add_f32 v[170:171], v[170:171], v[202:203]\n"
    "v_pk_add_f32 v[172:173], v[172:173], v[204:205]\n"
    "v_pk_add_f32 v[174:175], v[174:175], v[206:207]\n"
    "v_pk_add_f32 v[208:209], v[160:161], v[162:163]\n"
    "v_pk_add_f32 v[210:211], v[164:165], v[166:167]\n"
    "v_pk_add_f32 v[212:213], v[168:169], v[170:171]\n"
    "v_pk_add_f32 v[214:215], v[172:173], v[174:175]\n"
    "v_pk_mul_f32 v[160:161], v[160:161], v[162:163]\n"
    "v_pk_mul_f32 v[164:165], v[164:165], v[166:167]\n"
    "v_pk_mul_f32 v[168:169], v[168:169], v[170:171]\n"
    "v_pk_mul_f32 v[172:173], v[172:173], v[174:175]\n"
    "v_pk_mul_f32 v[208:209], v[208:209], v[164:165]\n"
    "v_pk_mul_f32 v[212:213], v[212:213], v[172:173]\n"
    "v_pk_mul_f32 v[216:217], v[160:161], v[164:165]\n"
    "v_pk_mul_f32 v[218:219], v[168:169], v[172:173]\n"
    "v_pk_fma_f32 v[208:209], v[210:211], v[160:161], v[208:209]\n"
    "v_pk_fma_f32 v[212:213], v[214:215], v[168:169], v[212:213]\n"
    "v_mfma_f32_32x32x16_bf16 v[160:175], v[0:3], a[32:35], v[96:111]\n"
    "s_waitcnt vmcnt(4) lgkmcnt(0)\n"
    "s_barrier\n"
    "ds_read_b128 a[32:35], v226 offset:28672\n"
    "ds_read_u16_d16_hi v225, v238 offset:41024\n"
    "v_rcp_f32 v216, v216\n"
    "v_mfma_f32_32x32x16_bf16 v[160:175], v[4:7], a[36:39], v[160:175]\n"
    "ds_read_b128 a[36:39], v227 offset:28672\n"
    "ds_read_b128 a[64:67], v234 offset:128\n"
    "v_add_f32 v224, v220, v221\n"
    "v_rcp_f32 v217, v217\n"
    "v_rcp_f32 v218, v218\n"
    "v_mfma_f32_32x32x16_bf16 v[160:175], v[8:11], a[40:43], v[160:175]\n"
    "ds_read_b128 a[40:43], v226 offset:29184\n"
    "ds_read_b64_tr_b16 a[68:69], v228 offset:18432\n"
    "v_rcp_f32 v219, v219\n"
    "v_exp_f32_e64 v176, v176 clamp\n"
    "v_mfma_f32_32x32x16_bf16 v[160:175], v[12:15], a[44:47], v[160:175]\n"
    "ds_read_b128 a[44:47], v227 offset:29184\n"
    "ds_read_b64_tr_b16 a[70:71], v229 offset:18432\n"
    "s_waitcnt lgkmcnt(6)\n"
    "v_mul_f32 v224, v224, v225\n"
    "v_cvt_pk_bf16_f32 v224, v224, v224\n"
    "v_exp_f32_e64 v177, v177 clamp\n"
    "v_exp_f32_e64 v178, v178 clamp\n"
    "v_mfma_f32_32x32x16_bf16 v[160:175], v[16:19], a[48:51], v[160:175]\n"
    "ds_read_b128 a[48:51], v226 offset:29696\n"
    "ds_read_b64_tr_b16 a[72:73], v230 offset:18432\n"
    "v_exp_f32_e64 v179, v179 clamp\n"
    "v_exp_f32_e64 v180, v180 clamp\n"
    "v_mfma_f32_32x32x16_bf16 v[160:175], v[20:23], a[52:55], v[160:175]\n"
    "ds_read_b128 a[52:55], v227 offset:29696\n"
    "ds_read_b64_tr_b16 a[74:75], v231 offset:18432\n"
    "v_exp_f32_e64 v181, v181 clamp\n"
    "v_exp_f32_e64 v182, v182 clamp\n"
    "v_mfma_f32_32x32x16_bf16 v[160:175], v[24:27], a[56:59], v[160:175]\n"
    "ds_read_b128 a[56:59], v226 offset:30208\n"
    "ds_write_b16 v232, v224 offset:41088\n"
    "v_exp_f32_e64 v183, v183 clamp\n"
    "v_exp_f32_e64 v184, v184 clamp\n"
    "v_mfma_f32_32x32x16_bf16 v[160:175], v[28:31], a[60:63], v[160:175]\n"
    "s_waitcnt lgkmcnt(2)\n"
    "ds_read_b128 a[60:63], v227 offset:30208\n"
    "v_exp_f32_e64 v185, v185 clamp\n"
    "v_exp_f32_e64 v186, v186 clamp\n"
    "v_exp_f32_e64 v187, v187 clamp\n"
    "v_mfma_f32_16x16x32_bf16 a[76:79], a[64:67], a[68:71], a[76:79]\n"
    "v_exp_f32_e64 v188, v188 clamp\n"
    "v_exp_f32_e64 v189, v189 clamp\n"
    "v_mfma_f32_16x16x32_bf16 a[80:83], a[64:67], a[72:75], a[80:83]\n"
    "v_exp_f32_e64 v190, v190 clamp\n"
    "v_exp_f32_e64 v191, v191 clamp\n"
    "v_pk_fma_f32 v[220:221], v[208:209], v[216:217], v[222:223]\n"
    "v_pk_add_f32 v[176:177], v[176:177], v[192:193]\n"
    "v_pk_add_f32 v[178:179], v[178:179], v[194:195]\n"
    "v_pk_add_f32 v[180:181], v[180:181], v[196:197]\n"
    "v_pk_add_f32 v[182:183], v[182:183], v[198:199]\n"
    "v_pk_fma_f32 v[220:221], v[212:213], v[218:219], v[220:221]\n"
    "v_pk_add_f32 v[184:185], v[184:185], v[200:201]\n"
    "v_pk_add_f32 v[186:187], v[186:187], v[202:203]\n"
    "v_pk_add_f32 v[188:189], v[188:189], v[204:205]\n"
    "v_pk_add_f32 v[190:191], v[190:191], v[206:207]\n"
    "v_pk_add_f32 v[208:209], v[176:177], v[178:179]\n"
    "v_pk_add_f32 v[210:211], v[180:181], v[182:183]\n"
    "v_pk_add_f32 v[212:213], v[184:185], v[186:187]\n"
    "v_pk_add_f32 v[214:215], v[188:189], v[190:191]\n"
    "v_pk_mul_f32 v[176:177], v[176:177], v[178:179]\n"
    "v_pk_mul_f32 v[180:181], v[180:181], v[182:183]\n"
    "v_pk_mul_f32 v[184:185], v[184:185], v[186:187]\n"
    "v_pk_mul_f32 v[188:189], v[188:189], v[190:191]\n"
    "v_pk_mul_f32 v[208:209], v[208:209], v[180:181]\n"
    "v_pk_mul_f32 v[212:213], v[212:213], v[188:189]\n"
    "v_pk_mul_f32 v[216:217], v[176:177], v[180:181]\n"
    "v_pk_mul_f32 v[218:219], v[184:185], v[188:189]\n"
    "v_pk_fma_f32 v[208:209], v[210:211], v[176:177], v[208:209]\n"
    "v_pk_fma_f32 v[212:213], v[214:215], v[184:185], v[212:213]\n"
    "v_mfma_f32_32x32x16_bf16 v[176:191], v[32:35], a[32:35], v[112:127]\n"
    "s_waitcnt vmcnt(2) lgkmcnt(0)\n"
    "s_barrier\n"
    "ds_read_b128 a[32:35], v226 offset:30720\n"
    "ds_read_u16_d16_hi v225, v239 offset:64\n"
    "v_rcp_f32 v216, v216\n"
    "v_mfma_f32_32x32x16_bf16 v[176:191], v[36:39], a[36:39], v[176:191]\n"
    "ds_read_b128 a[36:39], v227 offset:30720\n"
    "ds_read_b128 a[64:67], v235 offset:128\n"
    "v_add_f32 v224, v220, v221\n"
    "v_rcp_f32 v217, v217\n"
    "v_rcp_f32 v218, v218\n"
    "v_mfma_f32_32x32x16_bf16 v[176:191], v[40:43], a[40:43], v[176:191]\n"
    "ds_read_b128 a[40:43], v226 offset:31232\n"
    "ds_read_b64_tr_b16 a[68:69], v228 offset:20480\n"
    "v_rcp_f32 v219, v219\n"
    "v_exp_f32_e64 v160, v160 clamp\n"
    "v_mfma_f32_32x32x16_bf16 v[176:191], v[44:47], a[44:47], v[176:191]\n"
    "ds_read_b128 a[44:47], v227 offset:31232\n"
    "ds_read_b64_tr_b16 a[70:71], v229 offset:20480\n"
    "s_waitcnt lgkmcnt(6)\n"
    "v_mul_f32 v224, v224, v225\n"
    "v_cvt_pk_bf16_f32 v224, v224, v224\n"
    "v_exp_f32_e64 v161, v161 clamp\n"
    "v_exp_f32_e64 v162, v162 clamp\n"
    "v_mfma_f32_32x32x16_bf16 v[176:191], v[48:51], a[48:51], v[176:191]\n"
    "ds_read_b128 a[48:51], v226 offset:31744\n"
    "ds_read_b64_tr_b16 a[72:73], v230 offset:20480\n"
    "v_exp_f32_e64 v163, v163 clamp\n"
    "v_exp_f32_e64 v164, v164 clamp\n"
    "v_mfma_f32_32x32x16_bf16 v[176:191], v[52:55], a[52:55], v[176:191]\n"
    "ds_read_b128 a[52:55], v227 offset:31744\n"
    "ds_read_b64_tr_b16 a[74:75], v231 offset:20480\n"
    "v_exp_f32_e64 v165, v165 clamp\n"
    "v_exp_f32_e64 v166, v166 clamp\n"
    "v_mfma_f32_32x32x16_bf16 v[176:191], v[56:59], a[56:59], v[176:191]\n"
    "ds_read_b128 a[56:59], v226 offset:32256\n"
    "ds_write_b16 v233, v224 offset:128\n"
    "v_exp_f32_e64 v167, v167 clamp\n"
    "v_exp_f32_e64 v168, v168 clamp\n"
    "v_mfma_f32_32x32x16_bf16 v[176:191], v[60:63], a[60:63], v[176:191]\n"
    "s_waitcnt lgkmcnt(2)\n"
    "ds_read_b128 a[60:63], v227 offset:32256\n"
    "v_exp_f32_e64 v169, v169 clamp\n"
    "v_exp_f32_e64 v170, v170 clamp\n"
    "v_exp_f32_e64 v171, v171 clamp\n"
    "v_mfma_f32_16x16x32_bf16 a[76:79], a[64:67], a[68:71], a[76:79]\n"
    "v_exp_f32_e64 v172, v172 clamp\n"
    "v_exp_f32_e64 v173, v173 clamp\n"
    "v_mfma_f32_16x16x32_bf16 a[80:83], a[64:67], a[72:75], a[80:83]\n"
    "v_exp_f32_e64 v174, v174 clamp\n"
    "v_exp_f32_e64 v175, v175 clamp\n"
    "v_pk_fma_f32 v[220:221], v[208:209], v[216:217], v[222:223]\n"
    "v_pk_add_f32 v[160:161], v[160:161], v[192:193]\n"
    "v_pk_add_f32 v[162:163], v[162:163], v[194:195]\n"
    "v_pk_add_f32 v[164:165], v[164:165], v[196:197]\n"
    "v_pk_add_f32 v[166:167], v[166:167], v[198:199]\n"
    "v_pk_fma_f32 v[220:221], v[212:213], v[218:219], v[220:221]\n"
    "v_pk_add_f32 v[168:169], v[168:169], v[200:201]\n"
    "v_pk_add_f32 v[170:171], v[170:171], v[202:203]\n"
    "v_pk_add_f32 v[172:173], v[172:173], v[204:205]\n"
    "v_pk_add_f32 v[174:175], v[174:175], v[206:207]\n"
    "v_pk_add_f32 v[208:209], v[160:161], v[162:163]\n"
    "v_pk_add_f32 v[210:211], v[164:165], v[166:167]\n"
    "v_pk_add_f32 v[212:213], v[168:169], v[170:171]\n"
    "v_pk_add_f32 v[214:215], v[172:173], v[174:175]\n"
    "v_pk_mul_f32 v[160:161], v[160:161], v[162:163]\n"
    "v_pk_mul_f32 v[164:165], v[164:165], v[166:167]\n"
    "v_pk_mul_f32 v[168:169], v[168:169], v[170:171]\n"
    "v_pk_mul_f32 v[172:173], v[172:173], v[174:175]\n"
    "v_pk_mul_f32 v[208:209], v[208:209], v[164:165]\n"
    "v_pk_mul_f32 v[212:213], v[212:213], v[172:173]\n"
    "v_pk_mul_f32 v[216:217], v[160:161], v[164:165]\n"
    "v_pk_mul_f32 v[218:219], v[168:169], v[172:173]\n"
    "v_pk_fma_f32 v[208:209], v[210:211], v[160:161], v[208:209]\n"
    "v_pk_fma_f32 v[212:213], v[214:215], v[168:169], v[212:213]\n"
    "v_mfma_f32_32x32x16_bf16 v[160:175], v[64:67], a[32:35], v[128:143]\n"
    "s_waitcnt vmcnt(0) lgkmcnt(0)\n"
    "s_barrier\n"
    "ds_read_b128 a[32:35], v226 offset:6144\n"
    "ds_read_u16_d16_hi v225, v239 offset:41024\n"
    "v_rcp_f32 v216, v216\n"
    "v_mfma_f32_32x32x16_bf16 v[160:175], v[68:71], a[36:39], v[160:175]\n"
    "ds_read_b128 a[36:39], v227 offset:6144\n"
    "ds_read_b128 a[64:67], v236 offset:128\n"
    "v_add_f32 v224, v220, v221\n"
    "v_rcp_f32 v217, v217\n"
    "v_rcp_f32 v218, v218\n"
    "v_mfma_f32_32x32x16_bf16 v[160:175], v[72:75], a[40:43], v[160:175]\n"
    "ds_read_b128 a[40:43], v226 offset:6656\n"
    "ds_read_b64_tr_b16 a[68:69], v228 offset:22528\n"
    "v_rcp_f32 v219, v219\n"
    "v_exp_f32_e64 v176, v176 clamp\n"
    "v_mfma_f32_32x32x16_bf16 v[160:175], v[76:79], a[44:47], v[160:175]\n"
    "ds_read_b128 a[44:47], v227 offset:6656\n"
    "ds_read_b64_tr_b16 a[70:71], v229 offset:22528\n"
    "s_waitcnt lgkmcnt(6)\n"
    "v_mul_f32 v224, v224, v225\n"
    "v_cvt_pk_bf16_f32 v224, v224, v224\n"
    "v_exp_f32_e64 v177, v177 clamp\n"
    "v_exp_f32_e64 v178, v178 clamp\n"
    "v_mfma_f32_32x32x16_bf16 v[160:175], v[80:83], a[48:51], v[160:175]\n"
    "ds_read_b128 a[48:51], v226 offset:7168\n"
    "ds_read_b64_tr_b16 a[72:73], v230 offset:22528\n"
    "v_exp_f32_e64 v179, v179 clamp\n"
    "v_exp_f32_e64 v180, v180 clamp\n"
    "v_mfma_f32_32x32x16_bf16 v[160:175], v[84:87], a[52:55], v[160:175]\n"
    "ds_read_b128 a[52:55], v227 offset:7168\n"
    "ds_read_b64_tr_b16 a[74:75], v231 offset:22528\n"
    "v_exp_f32_e64 v181, v181 clamp\n"
    "v_exp_f32_e64 v182, v182 clamp\n"
    "v_mfma_f32_32x32x16_bf16 v[160:175], v[88:91], a[56:59], v[160:175]\n"
    "ds_read_b128 a[56:59], v226 offset:7680\n"
    "ds_write_b16 v233, v224 offset:41088\n"
    "v_exp_f32_e64 v183, v183 clamp\n"
    "v_exp_f32_e64 v184, v184 clamp\n"
    "v_mfma_f32_32x32x16_bf16 v[160:175], v[92:95], a[60:63], v[160:175]\n"
    "s_waitcnt lgkmcnt(2)\n"
    "ds_read_b128 a[60:63], v227 offset:7680\n"
    "v_exp_f32_e64 v185, v185 clamp\n"
    "v_exp_f32_e64 v186, v186 clamp\n"
    "v_exp_f32_e64 v187, v187 clamp\n"
    "v_mfma_f32_16x16x32_bf16 a[84:87], a[64:67], a[68:71], a[84:87]\n"
    "v_exp_f32_e64 v188, v188 clamp\n"
    "v_exp_f32_e64 v189, v189 clamp\n"
    "v_mfma_f32_16x16x32_bf16 a[88:91], a[64:67], a[72:75], a[88:91]\n"
    "v_exp_f32_e64 v190, v190 clamp\n"
    "v_exp_f32_e64 v191, v191 clamp\n"
    "v_pk_fma_f32 v[220:221], v[208:209], v[216:217], v[222:223]\n"
    "v_pk_add_f32 v[176:177], v[176:177], v[192:193]\n"
    "v_pk_add_f32 v[178:179], v[178:179], v[194:195]\n"
    "v_pk_add_f32 v[180:181], v[180:181], v[196:197]\n"
    "v_pk_add_f32 v[182:183], v[182:183], v[198:199]\n"
    "v_pk_fma_f32 v[220:221], v[212:213], v[218:219], v[220:221]\n"
    "v_pk_add_f32 v[184:185], v[184:185], v[200:201]\n"
    "v_pk_add_f32 v[186:187], v[186:187], v[202:203]\n"
    "v_pk_add_f32 v[188:189], v[188:189], v[204:205]\n"
    "v_pk_add_f32 v[190:191], v[190:191], v[206:207]\n"
    "v_pk_add_f32 v[208:209], v[176:177], v[178:179]\n"
    "v_pk_add_f32 v[210:211], v[180:181], v[182:183]\n"
    "v_pk_add_f32 v[212:213], v[184:185], v[186:187]\n"
    "v_pk_add_f32 v[214:215], v[188:189], v[190:191]\n"
    "v_pk_mul_f32 v[176:177], v[176:177], v[178:179]\n"
    "v_pk_mul_f32 v[180:181], v[180:181], v[182:183]\n"
    "v_pk_mul_f32 v[184:185], v[184:185], v[186:187]\n"
    "v_pk_mul_f32 v[188:189], v[188:189], v[190:191]\n"
    "v_pk_mul_f32 v[208:209], v[208:209], v[180:181]\n"
    "v_pk_mul_f32 v[212:213], v[212:213], v[188:189]\n"
    "v_pk_mul_f32 v[216:217], v[176:177], v[180:181]\n"
    "v_pk_mul_f32 v[218:219], v[184:185], v[188:189]\n"
    "v_pk_fma_f32 v[208:209], v[210:211], v[176:177], v[208:209]\n"
    "v_pk_fma_f32 v[212:213], v[214:215], v[184:185], v[212:213]\n"
    "v_mfma_f32_32x32x16_bf16 v[176:191], a[0:3], a[32:35], v[144:159]\n"
    "s_waitcnt lgkmcnt(0)\n"
    "s_barrier\n"
    "ds_read_u16_d16_hi v225, v238 offset:128\n"
    "v_rcp_f32 v216, v216\n"
    "v_mfma_f32_32x32x16_bf16 v[176:191], a[4:7], a[36:39], v[176:191]\n"
    "ds_read_b128 a[64:67], v237 offset:128\n"
    "v_add_f32 v224, v220, v221\n"
    "v_rcp_f32 v217, v217\n"
    "v_rcp_f32 v218, v218\n"
    "v_mfma_f32_32x32x16_bf16 v[176:191], a[8:11], a[40:43], v[176:191]\n"
    "ds_read_b64_tr_b16 a[68:69], v228 offset:24576\n"
    "v_rcp_f32 v219, v219\n"
    "v_exp_f32_e64 v160, v160 clamp\n"
    "v_mfma_f32_32x32x16_bf16 v[176:191], a[12:15], a[44:47], v[176:191]\n"
    "ds_read_b64_tr_b16 a[70:71], v229 offset:24576\n"
    "s_waitcnt lgkmcnt(3)\n"
    "v_mul_f32 v224, v224, v225\n"
    "v_cvt_pk_bf16_f32 v224, v224, v224\n"
    "v_exp_f32_e64 v161, v161 clamp\n"
    "v_exp_f32_e64 v162, v162 clamp\n"
    "v_mfma_f32_32x32x16_bf16 v[176:191], a[16:19], a[48:51], v[176:191]\n"
    "ds_read_b64_tr_b16 a[72:73], v230 offset:24576\n"
    "v_exp_f32_e64 v163, v163 clamp\n"
    "v_exp_f32_e64 v164, v164 clamp\n"
    "v_mfma_f32_32x32x16_bf16 v[176:191], a[20:23], a[52:55], v[176:191]\n"
    "ds_read_b64_tr_b16 a[74:75], v231 offset:24576\n"
    "v_exp_f32_e64 v165, v165 clamp\n"
    "v_exp_f32_e64 v166, v166 clamp\n"
    "v_mfma_f32_32x32x16_bf16 v[176:191], a[24:27], a[56:59], v[176:191]\n"
    "ds_write_b16 v232, v224 offset:192\n"
    "v_exp_f32_e64 v167, v167 clamp\n"
    "v_exp_f32_e64 v168, v168 clamp\n"
    "v_mfma_f32_32x32x16_bf16 v[176:191], a[28:31], a[60:63], v[176:191]\n"
    "s_waitcnt lgkmcnt(1)\n"
    "v_exp_f32_e64 v169, v169 clamp\n"
    "v_exp_f32_e64 v170, v170 clamp\n"
    "v_exp_f32_e64 v171, v171 clamp\n"
    "v_mfma_f32_16x16x32_bf16 a[84:87], a[64:67], a[68:71], a[84:87]\n"
    "v_exp_f32_e64 v172, v172 clamp\n"
    "v_exp_f32_e64 v173, v173 clamp\n"
    "v_mfma_f32_16x16x32_bf16 a[88:91], a[64:67], a[72:75], a[88:91]\n"
    "v_exp_f32_e64 v174, v174 clamp\n"
    "v_exp_f32_e64 v175, v175 clamp\n"
    "v_pk_fma_f32 v[220:221], v[208:209], v[216:217], v[222:223]\n"
    "v_pk_add_f32 v[160:161], v[160:161], v[192:193]\n"
    "v_pk_add_f32 v[162:163], v[162:163], v[194:195]\n"
    "v_pk_add_f32 v[164:165], v[164:165], v[196:197]\n"
    "v_pk_add_f32 v[166:167], v[166:167], v[198:199]\n"
    "v_pk_fma_f32 v[220:221], v[212:213], v[218:219], v[220:221]\n"
    "v_pk_add_f32 v[168:169], v[168:169], v[200:201]\n"
    "v_pk_add_f32 v[170:171], v[170:171], v[202:203]\n"
    "v_pk_add_f32 v[172:173], v[172:173], v[204:205]\n"
    "v_pk_add_f32 v[174:175], v[174:175], v[206:207]\n"
    "v_pk_add_f32 v[208:209], v[160:161], v[162:163]\n"
    "v_pk_add_f32 v[210:211], v[164:165], v[166:167]\n"
    "v_pk_add_f32 v[212:213], v[168:169], v[170:171]\n"
    "v_pk_add_f32 v[214:215], v[172:173], v[174:175]\n"
    "v_pk_mul_f32 v[160:161], v[160:161], v[162:163]\n"
    "v_pk_mul_f32 v[164:165], v[164:165], v[166:167]\n"
    "v_pk_mul_f32 v[168:169], v[168:169], v[170:171]\n"
    "v_pk_mul_f32 v[172:173], v[172:173], v[174:175]\n"
    "v_pk_mul_f32 v[208:209], v[208:209], v[164:165]\n"
    "v_pk_mul_f32 v[212:213], v[212:213], v[172:173]\n"
    "v_pk_mul_f32 v[216:217], v[160:161], v[164:165]\n"
    "v_pk_mul_f32 v[218:219], v[168:169], v[172:173]\n"
    "v_pk_fma_f32 v[208:209], v[210:211], v[160:161], v[208:209]\n"
    "v_pk_fma_f32 v[212:213], v[214:215], v[168:169], v[212:213]\n"
    "s_waitcnt lgkmcnt(0)\n"
    "s_barrier\n"
    "ds_read_u16_d16_hi v225, v238 offset:41088\n"
    "ds_read_b128 a[64:67], v234 offset:192\n"
    "ds_read_b64_tr_b16 a[68:69], v228 offset:26624\n"
    "ds_read_b64_tr_b16 a[70:71], v229 offset:26624\n"
    "ds_read_b64_tr_b16 a[72:73], v230 offset:26624\n"
    "ds_read_b64_tr_b16 a[74:75], v231 offset:26624\n"
    "v_rcp_f32 v216, v216\n"
    "v_rcp_f32 v217, v217\n"
    "v_rcp_f32 v218, v218\n"
    "v_rcp_f32 v219, v219\n"
    "v_exp_f32_e64 v176, v176 clamp\n"
    "v_exp_f32_e64 v177, v177 clamp\n"
    "v_add_f32 v224, v220, v221\n"
    "s_waitcnt lgkmcnt(5)\n"
    "v_mul_f32 v224, v224, v225\n"
    "v_cvt_pk_bf16_f32 v224, v224, v224\n"
    "ds_write_b16 v232, v224 offset:41152\n"
    "s_waitcnt lgkmcnt(3)\n"
    "v_mfma_f32_16x16x32_bf16 a[76:79], a[64:67], a[68:71], a[76:79]\n"
    "s_waitcnt lgkmcnt(1)\n"
    "v_mfma_f32_16x16x32_bf16 a[80:83], a[64:67], a[72:75], a[80:83]\n"
    "v_exp_f32_e64 v178, v178 clamp\n"
    "v_exp_f32_e64 v179, v179 clamp\n"
    "v_exp_f32_e64 v180, v180 clamp\n"
    "v_exp_f32_e64 v181, v181 clamp\n"
    "v_exp_f32_e64 v182, v182 clamp\n"
    "v_exp_f32_e64 v183, v183 clamp\n"
    "v_exp_f32_e64 v184, v184 clamp\n"
    "v_exp_f32_e64 v185, v185 clamp\n"
    "v_exp_f32_e64 v186, v186 clamp\n"
    "v_exp_f32_e64 v187, v187 clamp\n"
    "v_exp_f32_e64 v188, v188 clamp\n"
    "v_exp_f32_e64 v189, v189 clamp\n"
    "v_exp_f32_e64 v190, v190 clamp\n"
    "v_exp_f32_e64 v191, v191 clamp\n"
    "v_pk_fma_f32 v[220:221], v[208:209], v[216:217], v[222:223]\n"
    "v_pk_add_f32 v[176:177], v[176:177], v[192:193]\n"
    "v_pk_add_f32 v[178:179], v[178:179], v[194:195]\n"
    "v_pk_add_f32 v[180:181], v[180:181], v[196:197]\n"
    "v_pk_add_f32 v[182:183], v[182:183], v[198:199]\n"
    "v_pk_fma_f32 v[220:221], v[212:213], v[218:219], v[220:221]\n"
    "v_pk_add_f32 v[184:185], v[184:185], v[200:201]\n"
    "v_pk_add_f32 v[186:187], v[186:187], v[202:203]\n"
    "v_pk_add_f32 v[188:189], v[188:189], v[204:205]\n"
    "v_pk_add_f32 v[190:191], v[190:191], v[206:207]\n"
    "v_pk_add_f32 v[208:209], v[176:177], v[178:179]\n"
    "v_pk_add_f32 v[210:211], v[180:181], v[182:183]\n"
    "v_pk_add_f32 v[212:213], v[184:185], v[186:187]\n"
    "v_pk_add_f32 v[214:215], v[188:189], v[190:191]\n"
    "v_pk_mul_f32 v[176:177], v[176:177], v[178:179]\n"
    "v_pk_mul_f32 v[180:181], v[180:181], v[182:183]\n"
    "v_pk_mul_f32 v[184:185], v[184:185], v[186:187]\n"
    "v_pk_mul_f32 v[188:189], v[188:189], v[190:191]\n"
    "v_pk_mul_f32 v[208:209], v[208:209], v[180:181]\n"
    "v_pk_mul_f32 v[212:213], v[212:213], v[188:189]\n"
    "v_pk_mul_f32 v[216:217], v[176:177], v[180:181]\n"
    "v_pk_mul_f32 v[218:219], v[184:185], v[188:189]\n"
    "v_pk_fma_f32 v[208:209], v[210:211], v[176:177], v[208:209]\n"
    "v_pk_fma_f32 v[212:213], v[214:215], v[184:185], v[212:213]\n"
    "s_waitcnt lgkmcnt(0)\n"
    "s_barrier\n"
    "ds_read_u16_d16_hi v225, v239 offset:128\n"
    "ds_read_b128 a[64:67], v235 offset:192\n"
    "ds_read_b64_tr_b16 a[68:69], v228 offset:28672\n"
    "ds_read_b64_tr_b16 a[70:71], v229 offset:28672\n"
    "ds_read_b64_tr_b16 a[72:73], v230 offset:28672\n"
    "ds_read_b64_tr_b16 a[74:75], v231 offset:28672\n"
    "v_rcp_f32 v216, v216\n"
    "v_rcp_f32 v217, v217\n"
    "v_rcp_f32 v218, v218\n"
    "v_rcp_f32 v219, v219\n"
    "v_add_f32 v224, v220, v221\n"
    "s_waitcnt lgkmcnt(5)\n"
    "v_mul_f32 v224, v224, v225\n"
    "v_cvt_pk_bf16_f32 v224, v224, v224\n"
    "ds_write_b16 v233, v224 offset:192\n"
    "ds_read_u16_d16_hi v225, v239 offset:41088\n"
    "v_pk_fma_f32 v[220:221], v[208:209], v[216:217], v[222:223]\n"
    "v_pk_fma_f32 v[220:221], v[212:213], v[218:219], v[220:221]\n"
    "v_add_f32 v224, v220, v221\n"
    "s_waitcnt lgkmcnt(0)\n"
    "v_mul_f32 v224, v224, v225\n"
    "v_cvt_pk_bf16_f32 v224, v224, v224\n"
    "ds_write_b16 v233, v224 offset:41152\n"
    "v_mfma_f32_16x16x32_bf16 a[76:79], a[64:67], a[68:71], a[76:79]\n"
    "v_mfma_f32_16x16x32_bf16 a[80:83], a[64:67], a[72:75], a[80:83]\n"
    "s_waitcnt lgkmcnt(0)\n"
    "s_barrier\n"
    "ds_read_b128 a[64:67], v236 offset:192\n"
    "ds_read_b64_tr_b16 a[68:69], v228 offset:30720\n"
    "ds_read_b64_tr_b16 a[70:71], v229 offset:30720\n"
    "ds_read_b64_tr_b16 a[72:73], v230 offset:30720\n"
    "ds_read_b64_tr_b16 a[74:75], v231 offset:30720\n"
    "s_waitcnt lgkmcnt(2)\n"
    "v_mfma_f32_16x16x32_bf16 a[84:87], a[64:67], a[68:71], a[84:87]\n"
    "s_waitcnt lgkmcnt(0)\n"
    "v_mfma_f32_16x16x32_bf16 a[88:91], a[64:67], a[72:75], a[88:91]\n"
    "ds_read_b128 a[64:67], v237 offset:192\n"
    "ds_read_b64_tr_b16 a[68:69], v228 offset:6144\n"
    "ds_read_b64_tr_b16 a[70:71], v229 offset:6144\n"
    "ds_read_b64_tr_b16 a[72:73], v230 offset:6144\n"
    "ds_read_b64_tr_b16 a[74:75], v231 offset:6144\n"
    "s_waitcnt lgkmcnt(2)\n"
    "v_mfma_f32_16x16x32_bf16 a[84:87], a[64:67], a[68:71], a[84:87]\n"
    "s_waitcnt lgkmcnt(0)\n"
    "v_mfma_f32_16x16x32_bf16 a[88:91], a[64:67], a[72:75], a[88:91]\n"
    "s_and_b32 s46, s35, 0x02020202\n"
    "s_cbranch_scc0 L_gdone_%=\n"
    "s_barrier\n"
    "s_mov_b32 s35, 0\n"
    "v_add_u32 v238, 0x100, v238\n"
    "v_add_u32 v239, 0x100, v239\n"
    "v_accvgpr_read_b32 v242, a156\n"
    "v_accvgpr_read_b32 v243, a157\n"
    "v_accvgpr_read_b32 v244, a158\n"
    "v_accvgpr_read_b32 v245, a159\n"
    "s_add_u32 m0, s80, 0x0\n"
    "v_perm_b32 v246, v242, v241, s60\n"
    "buffer_load_dwordx4 v246, s[84:87], s48 offen offset:0 nt lds\n"
    "buffer_load_dwordx4 v246, s[84:87], s49 offen offset:1024 nt lds\n"
    "v_perm_b32 v247, v243, v241, s60\n"
    "buffer_load_dwordx4 v247, s[88:91], s50 offen offset:2048 nt lds\n"
    "buffer_load_dwordx4 v247, s[88:91], s51 offen offset:3072 nt lds\n"
    "s_add_u32 m0, s80, 0x1000\n"
    "v_perm_b32 v246, v244, v241, s60\n"
    "buffer_load_dwordx4 v246, s[92:95], s48 offen offset:0 nt lds\n"
    "buffer_load_dwordx4 v246, s[92:95], s49 offen offset:1024 nt lds\n"
    "s_add_u32 m0, s80, 0x2000\n"
    "v_perm_b32 v247, v245, v241, s60\n"
    "buffer_load_dwordx4 v247, s[96:99], s48 offen offset:0 nt lds\n"
    "buffer_load_dwordx4 v247, s[96:99], s49 offen offset:1024 nt lds\n"
    "v_perm_b32 v246, v242, v241, s63\n"
    "buffer_load_dwordx4 v246, s[84:87], s50 offen offset:2048 nt lds\n"
    "buffer_load_dwordx4 v246, s[84:87], s51 offen offset:3072 nt lds\n"
    "s_add_u32 m0, s80, 0x3000\n"
    "v_perm_b32 v247, v243, v241, s63\n"
    "buffer_load_dwordx4 v247, s[88:91], s48 offen offset:0 nt lds\n"
    "buffer_load_dwordx4 v247, s[88:91], s49 offen offset:1024 nt lds\n"
    "s_nop 15\n"
    "s_branch L_main1_%=\n"
    "L_gdone_%=:\n"
    "v_accvgpr_read_b32 v96, a76\n"
    "v_accvgpr_read_b32 v97, a77\n"
    "v_accvgpr_read_b32 v98, a78\n"
    "v_accvgpr_read_b32 v99, a79\n"
    "v_accvgpr_read_b32 v100, a80\n"
    "v_accvgpr_read_b32 v101, a81\n"
    "v_accvgpr_read_b32 v102, a82\n"
    "v_accvgpr_read_b32 v103, a83\n"
    "v_accvgpr_read_b32 v104, a84\n"
    "v_accvgpr_read_b32 v105, a85\n"
    "v_accvgpr_read_b32 v106, a86\n"
    "v_accvgpr_read_b32 v107, a87\n"
    "v_accvgpr_read_b32 v108, a88\n"
    "v_accvgpr_read_b32 v109, a89\n"
    "v_accvgpr_read_b32 v110, a90\n"
    "v_accvgpr_read_b32 v111, a91\n"
    "v_pk_add_f32 v[96:97], v[96:97], v[98:99]\n"
    "v_pk_add_f32 v[100:101], v[100:101], v[102:103]\n"
    "v_pk_add_f32 v[104:105], v[104:105], v[106:107]\n"
    "v_pk_add_f32 v[108:109], v[108:109], v[110:111]\n"
    "v_add_f32 v96, v96, v97\n"
    "v_add_f32 v100, v100, v101\n"
    "v_add_f32 v104, v104, v105\n"
    "v_add_f32 v108, v108, v109\n"
    "v_permlane16_swap_b32 v96, v100\n"
    "s_nop 0\n"
    "v_permlane16_swap_b32 v104, v108\n"
    "v_add_f32 v96, v96, v100\n"
    "v_add_f32 v104, v104, v108\n"
    "v_mul_f32 v96, s64, v96\n"
    "v_mul_f32 v104, s64, v104\n"
    "v_cvt_pk_bf16_f32 v96, v96, v104\n"
    "buffer_store_short v96, v240, s[40:43], 0 offen\n"
    "buffer_store_short_d16_hi v96, v240, s[40:43], 0 offen offset:512\n"
    "L_end_%=:\n"
    :
    : "s"(__builtin_amdgcn_kernarg_segment_ptr()), "s"(bid), "s"(wave), "s"((unsigned)(size_t)smem)
    : "v0", "v1", "v2", "v3", "v4", "v5", "v6", "v7", "v8", "v9", "v10", "v11", "v12", "v13", "v14", "v15", "v16", "v17", "v18", "v19", "v20", "v21", "v22", "v23", "v24", "v25", "v26", "v27", "v28", "v29", "v30", "v31", "v32", "v33", "v34", "v35", "v36", "v37", "v38", "v39", "v40", "v41", "v42", "v43", "v44", "v45", "v46", "v47", "v48", "v49", "v50", "v51", "v52", "v53", "v54", "v55", "v56", "v57", "v58", "v59", "v60", "v61", "v62", "v63", "v64", "v65", "v66", "v67", "v68", "v69", "v70", "v71", "v72", "v73", "v74", "v75", "v76", "v77", "v78", "v79", "v80", "v81", "v82", "v83", "v84", "v85", "v86", "v87", "v88", "v89", "v90", "v91", "v92", "v93", "v94", "v95", "v96", "v97", "v98", "v99", "v100", "v101", "v102", "v103", "v104", "v105", "v106", "v107", "v108", "v109", "v110", "v111", "v112", "v113", "v114", "v115", "v116", "v117", "v118", "v119", "v120", "v121", "v122", "v123", "v124", "v125", "v126", "v127", "v128", "v129", "v130", "v131", "v132", "v133", "v134", "v135", "v136", "v137", "v138", "v139", "v140", "v141", "v142", "v143", "v144", "v145", "v146", "v147", "v148", "v149", "v150", "v151", "v152", "v153", "v154", "v155", "v156", "v157", "v158", "v159", "v160", "v161", "v162", "v163", "v164", "v165", "v166", "v167", "v168", "v169", "v170", "v171", "v172", "v173", "v174", "v175", "v176", "v177", "v178", "v179", "v180", "v181", "v182", "v183", "v184", "v185", "v186", "v187", "v188", "v189", "v190", "v191", "v192", "v193", "v194", "v195", "v196", "v197", "v198", "v199", "v200", "v201", "v202", "v203", "v204", "v205", "v206", "v207", "v208", "v209", "v210", "v211", "v212", "v213", "v214", "v215", "v216", "v217", "v218", "v219", "v220", "v221", "v222", "v223", "v224", "v225", "v226", "v227", "v228", "v229", "v230", "v231", "v232", "v233", "v234", "v235", "v236", "v237", "v238", "v239", "v240", "v241", "v242", "v243", "v244", "v245", "v246", "v247", "v248", "v249", "v250", "v251", "v252", "v253", "v254", "v255", "a0", "a1", "a2", "a3", "a4", "a5", "a6", "a7", "a8", "a9", "a10", "a11", "a12", "a13", "a14", "a15", "a16", "a17", "a18", "a19", "a20", "a21", "a22", "a23", "a24", "a25", "a26", "a27", "a28", "a29", "a30", "a31", "a32", "a33", "a34", "a35", "a36", "a37", "a38", "a39", "a40", "a41", "a42", "a43", "a44", "a45", "a46", "a47", "a48", "a49", "a50", "a51", "a52", "a53", "a54", "a55", "a56", "a57", "a58", "a59", "a60", "a61", "a62", "a63", "a64", "a65", "a66", "a67", "a68", "a69", "a70", "a71", "a72", "a73", "a74", "a75", "a76", "a77", "a78", "a79", "a80", "a81", "a82", "a83", "a84", "a85", "a86", "a87", "a88", "a89", "a90", "a91", "a92", "a93", "a94", "a95", "a96", "a97", "a98", "a99", "a100", "a101", "a102", "a103", "a104", "a105", "a106", "a107", "a108", "a109", "a110", "a111", "a112", "a113", "a114", "a115", "a116", "a117", "a118", "a119", "a120", "a121", "a122", "a123", "a124", "a125", "a126", "a127", "a128", "a129", "a130", "a131", "a132", "a133", "a134", "a135", "a136", "a137", "a138", "a139", "a140", "a141", "a142", "a143", "a144", "a145", "a146", "a147", "a148", "a149", "a150", "a151", "a152", "a153", "a154", "a155", "a156", "a157", "a158", "a159", "a160", "a161", "a162", "a163", "a164", "a165", "a166", "a167", "a168", "a169", "a170", "a171", "a172", "a173", "a174", "a175", "a176", "a177", "a178", "a179", "a180", "a181", "a182", "a183", "a184", "a185", "a186", "a187", "a188", "a189", "a190", "a191", "a192", "a193", "a194", "a195", "a196", "a197", "a198", "a199", "a200", "a201", "a202", "a203", "a204", "a205", "a206", "a207", "a208", "a209", "a210", "a211", "a212", "a213", "a214", "a215", "a216", "a217", "a218", "a219", "a220", "a221", "a222", "a223", "a224", "a225", "a226", "a227", "a228", "a229", "a230", "a231", "a232", "a233", "a234", "a235", "a236", "a237", "a238", "a239", "a240", "a241", "a242", "a243", "a244", "a245", "a246", "a247", "a248", "a249", "a250", "a251", "a252", "a253", "a254", "a255", "s8", "s9", "s10", "s11", "s12", "s13", "s14", "s15", "s16", "s17", "s18", "s19", "s20", "s21", "s22", "s23", "s24", "s25", "s26", "s27", "s28", "s29", "s30", "s31", "s33", "s34", "s35", "s36", "s37", "s38", "s39", "s40", "s41", "s42", "s43", "s44", "s45", "s46", "s47", "s48", "s49", "s50", "s51", "s52", "s53", "s54", "s55", "s56", "s57", "s58", "s59", "s60", "s61", "s62", "s63", "s64", "s65", "s66", "s67", "s68", "s69", "s70", "s71", "s72", "s73", "s74", "s75", "s76", "s77", "s78", "s79", "s80", "s81", "s82", "s83", "s84", "s85", "s86", "s87", "s88", "s89", "s90", "s91", "s92", "s93", "s94", "s95", "s96", "s97", "s98", "s99", "s100", "s101", "vcc", "scc", "memory");
}

extern "C" void kernel_launch(void* const* d_in, const int* in_sizes, int n_in,
                              void* d_out, int out_size, void* d_ws, size_t ws_size,
                              hipStream_t stream) {
  din_kernel<<<256, 256, 0, stream>>>(d_in[0], d_in[1], d_in[2], d_in[3], d_in[4], d_in[5], d_in[6], d_out);
}
```
